```python
import torch

def _softmax(x, axis=-1):
    return torch.softmax(x, dim=axis)
def _gelu(x):
    return torch.nn.functional.gelu(x, approximate="tanh")
def _silu(x):
    return torch.nn.functional.silu(x)
def _sigmoid(x):
    return torch.sigmoid(x)
def _layernorm(x, axis=-1, eps=1e-5):
    m = x.mean(dim=axis, keepdim=True)
    v = x.var(dim=axis, keepdim=True, unbiased=False)
    return (x - m) / torch.sqrt(v + eps)
def _scatter_add(tgt, idx, src, axis=0):
    return tgt.clone().index_add_(axis, idx, src)
def _segment_sum(src, seg, n):
    out = torch.zeros((n,) + src.shape[1:], dtype=src.dtype)
    return out.index_add_(0, seg, src)
def _pad(x, pw):
    flat = tuple(v for pair in reversed(pw) for v in pair)
    return torch.nn.functional.pad(x, flat)

kernel_name = 'standalone__gqa_softmax__near_ridge'

def setup_inputs():

    _w = (lambda a: (lambda t: t.to(torch.bfloat16) if t.is_floating_point() else t)(torch.as_tensor(a)))
    B, Hq, Hkv, Sq, Sk, D = 16, 32, 8, 496, 512, 128
    return {
        "q": _w((lambda *s: torch.randn(*s, dtype=torch.bfloat16) * 0.8)(B, Hq, Sq, D)),
        "k": _w((lambda *s: torch.randn(*s, dtype=torch.bfloat16) * 0.8)(B, Hkv, Sk, D)),
        "v": _w((lambda *s: torch.randn(*s, dtype=torch.bfloat16) * 0.05)(B, Hkv, Sk, D)),
    }

def reference(q, k, v):
    B, Hq, Sq, D = q.shape
    Hkv = k.shape[1]
    rep = Hq // Hkv
    k = (lambda x, n, axis: torch.repeat_interleave(x, n, dim=axis))(k, rep, axis=1)
    v = (lambda x, n, axis: torch.repeat_interleave(x, n, dim=axis))(v, rep, axis=1)
    scale = D ** -0.5
    scores = torch.einsum("bhqd,bhkd->bhqk", q, k) * scale
    p = _softmax(scores, axis=-1)
    return torch.einsum("bhqk,bhkd->bhqd", p, v)
```

```cpp
#include <hip/hip_runtime.h>
#include <hip/hip_bf16.h>
typedef __attribute__((ext_vector_type(8))) short bf16x8_t;
typedef __attribute__((ext_vector_type(16))) float f32x16_t;
typedef __attribute__((ext_vector_type(4))) unsigned u32x4_t;
typedef __attribute__((ext_vector_type(2))) float f32x2_t;
typedef __attribute__((ext_vector_type(2))) __bf16 bf16x2_t;

__device__ __forceinline__ unsigned pack_bf16(float lo, float hi) {
  f32x2_t v = {lo, hi};
  bf16x2_t b = __builtin_convertvector(v, bf16x2_t);
  return __builtin_bit_cast(unsigned, b);
}

__device__ __forceinline__ void exact_block(const unsigned short* __restrict__ Qb, const unsigned short* __restrict__ Kg,
                                            const unsigned short* __restrict__ Vg, unsigned short* __restrict__ Ob,
                                            char* lds, int w, int lane) {
  const int r = lane & 31, h = lane >> 5;
  const float C = 0.12751743082f;
  float* red_m = (float*)(lds + 32768);
  float* red_l = (float*)(lds + 32768 + 1024);
  f32x16_t s[4];
#pragma unroll
  for (int kb = 0; kb < 4; ++kb) {
    f32x16_t acc = {};
#pragma unroll
    for (int d0 = 0; d0 < 8; ++d0) {
      const bf16x8_t a = *(const bf16x8_t*)(Kg + (128 * w + 32 * kb + r) * 128 + 16 * d0 + 8 * h);
      const bf16x8_t b = *(const bf16x8_t*)(Qb + r * 128 + 16 * d0 + 8 * h);
      acc = __builtin_amdgcn_mfma_f32_32x32x16_bf16(a, b, acc, 0, 0, 0);
    }
    s[kb] = acc;
  }
  float m = s[0][0];
#pragma unroll
  for (int kb = 0; kb < 4; ++kb)
#pragma unroll
    for (int i = 0; i < 16; ++i) m = fmaxf(m, s[kb][i]);
  red_m[(2 * w + h) * 32 + r] = m;
  __syncthreads();
#pragma unroll
  for (int i = 0; i < 8; ++i) m = fmaxf(m, red_m[i * 32 + r]);
  float l = 0.f;
#pragma unroll
  for (int kb = 0; kb < 4; ++kb) {
#pragma unroll
    for (int i = 0; i < 16; ++i) { s[kb][i] = __builtin_amdgcn_exp2f((s[kb][i] - m) * C); l += s[kb][i]; }
#pragma unroll
    for (int st = 0; st < 2; ++st) {
      u32x4_t p;
#pragma unroll
      for (int j = 0; j < 4; ++j) p[j] = pack_bf16(s[kb][8 * st + 2 * j], s[kb][8 * st + 2 * j + 1]);
      *(u32x4_t*)(lds + ((8 * w + 2 * kb + st) * 64 + lane) * 16) = p;
    }
  }
  red_l[(2 * w + h) * 32 + r] = l;
  __syncthreads();
  l = 0.f;
#pragma unroll
  for (int i = 0; i < 8; ++i) l += red_l[i * 32 + r];
  f32x16_t o = {};
#pragma unroll 2
  for (int f = 0; f < 32; ++f) {
    bf16x8_t a;
#pragma unroll
    for (int j = 0; j < 8; ++j) a[j] = (short)Vg[(16 * f + 8 * (j >> 2) + 4 * h + (j & 3)) * 128 + 32 * w + r];
    const bf16x8_t b = *(const bf16x8_t*)(lds + (f * 64 + lane) * 16);
    o = __builtin_amdgcn_mfma_f32_32x32x16_bf16(a, b, o, 0, 0, 0);
  }
  const float inv = 1.0f / l;
#pragma unroll
  for (int g = 0; g < 4; ++g) {
    uint2 v;
    v.x = pack_bf16(o[4 * g + 0] * inv, o[4 * g + 1] * inv);
    v.y = pack_bf16(o[4 * g + 2] * inv, o[4 * g + 3] * inv);
    *(uint2*)(Ob + r * 128 + 32 * w + 8 * g + 4 * h) = v;
  }
  __syncthreads();
}
__global__ __launch_bounds__(256) void attn_exact(const unsigned short* __restrict__ Q, const unsigned short* __restrict__ K,
                                                  const unsigned short* __restrict__ V, unsigned short* __restrict__ O) {
  __shared__ __attribute__((aligned(16))) char lds[32768 + 2048];
  const int blk = blockIdx.x;
  const int group = blk / 62;
  exact_block(Q + (size_t)blk * 32 * 128, K + (size_t)group * 512 * 128, V + (size_t)group * 512 * 128, O + (size_t)blk * 32 * 128,
              lds, threadIdx.x >> 6, threadIdx.x & 63);
}
extern "C" void kernel_launch(void* const* d_in, const int* in_sizes, int n_in, void* d_out, int out_size, void* d_ws, size_t ws_size,
                              hipStream_t stream) {
  attn_exact<<<7936, 256, 0, stream>>>((const unsigned short*)d_in[0], (const unsigned short*)d_in[1], (const unsigned short*)d_in[2],
                                       (unsigned short*)d_out);
}
```

```cpp
#include <hip/hip_runtime.h>
#include <hip/hip_bf16.h>

typedef __attribute__((ext_vector_type(8))) short bf16x8_t;
typedef __attribute__((ext_vector_type(16))) float f32x16_t;
typedef __attribute__((ext_vector_type(4))) unsigned u32x4_t;
typedef __attribute__((ext_vector_type(2))) float f32x2_t;
typedef __attribute__((ext_vector_type(2))) __bf16 bf16x2_t;

__device__ __forceinline__ unsigned pack_bf16(float lo, float hi) {
  f32x2_t v = {lo, hi};
  bf16x2_t b = __builtin_convertvector(v, bf16x2_t);
  return __builtin_bit_cast(unsigned, b);
}

__device__ __forceinline__ void exact_block(const unsigned short* __restrict__ Qb, const unsigned short* __restrict__ Kg,
                                            const unsigned short* __restrict__ Vg, unsigned short* __restrict__ Ob,
                                            char* lds, int w, int lane) {
  const int r = lane & 31, h = lane >> 5;
  const float C = 0.12751743082f;
  float* red_m = (float*)(lds + 32768);
  float* red_l = (float*)(lds + 32768 + 1024);
  f32x16_t s[4];
#pragma unroll
  for (int kb = 0; kb < 4; ++kb) {
    f32x16_t acc = {};
#pragma unroll
    for (int d0 = 0; d0 < 8; ++d0) {
      const bf16x8_t a = *(const bf16x8_t*)(Kg + (128 * w + 32 * kb + r) * 128 + 16 * d0 + 8 * h);
      const bf16x8_t b = *(const bf16x8_t*)(Qb + r * 128 + 16 * d0 + 8 * h);
      acc = __builtin_amdgcn_mfma_f32_32x32x16_bf16(a, b, acc, 0, 0, 0);
    }
    s[kb] = acc;
  }
  float m = s[0][0];
#pragma unroll
  for (int kb = 0; kb < 4; ++kb)
#pragma unroll
    for (int i = 0; i < 16; ++i) m = fmaxf(m, s[kb][i]);
  red_m[(2 * w + h) * 32 + r] = m;
  __syncthreads();
#pragma unroll
  for (int i = 0; i < 8; ++i) m = fmaxf(m, red_m[i * 32 + r]);
  float l = 0.f;
#pragma unroll
  for (int kb = 0; kb < 4; ++kb) {
#pragma unroll
    for (int i = 0; i < 16; ++i) { s[kb][i] = __builtin_amdgcn_exp2f((s[kb][i] - m) * C); l += s[kb][i]; }
#pragma unroll
    for (int st = 0; st < 2; ++st) {
      u32x4_t p;
#pragma unroll
      for (int j = 0; j < 4; ++j) p[j] = pack_bf16(s[kb][8 * st + 2 * j], s[kb][8 * st + 2 * j + 1]);
      *(u32x4_t*)(lds + ((8 * w + 2 * kb + st) * 64 + lane) * 16) = p;
    }
  }
  red_l[(2 * w + h) * 32 + r] = l;
  __syncthreads();
  l = 0.f;
#pragma unroll
  for (int i = 0; i < 8; ++i) l += red_l[i * 32 + r];
  f32x16_t o = {};
#pragma unroll 2
  for (int f = 0; f < 32; ++f) {
    bf16x8_t a;
#pragma unroll
    for (int j = 0; j < 8; ++j) a[j] = (short)Vg[(16 * f + 8 * (j >> 2) + 4 * h + (j & 3)) * 128 + 32 * w + r];
    const bf16x8_t b = *(const bf16x8_t*)(lds + (f * 64 + lane) * 16);
    o = __builtin_amdgcn_mfma_f32_32x32x16_bf16(a, b, o, 0, 0, 0);
  }
  const float inv = 1.0f / l;
#pragma unroll
  for (int g = 0; g < 4; ++g) {
    uint2 v;
    v.x = pack_bf16(o[4 * g + 0] * inv, o[4 * g + 1] * inv);
    v.y = pack_bf16(o[4 * g + 2] * inv, o[4 * g + 3] * inv);
    *(uint2*)(Ob + r * 128 + 32 * w + 8 * g + 4 * h) = v;
  }
  __syncthreads();
}


__global__ __launch_bounds__(256, 1) __attribute__((aligned(4096)))
void gqa_attn_kernel(const unsigned short* __restrict__ Q, const unsigned short* __restrict__ K,
                     const unsigned short* __restrict__ V, unsigned short* __restrict__ O
) {
  __shared__ __attribute__((aligned(1024))) char lds[163840];
  const unsigned w = __builtin_amdgcn_readfirstlane(threadIdx.x >> 6);
  unsigned mask;
  asm volatile(
      "s_load_dwordx8 s[12:19], %1, 0x0\n"
      "s_mov_b32 s68, %2\n"
      "s_and_b32 s69, s68, 7\n"
      "s_lshl_b32 s69, s69, 5\n"
      "s_lshr_b32 s68, s68, 3\n"
      "s_add_u32 s69, s69, s68\n"
      "s_mul_i32 s24, s69, 0x3e000\n"
      "s_lshr_b32 s69, s69, 1\n"
      "s_lshl_b32 s25, s69, 17\n"
      "s_mov_b32 s46, 0x20000\n"
      "s_mov_b32 s47, 0x20000\n"
      "s_mov_b32 s42, 0x3e000\n"
      "s_mov_b32 s43, 0x20000\n"
      "s_mov_b32 s50, 0x20000\n"
      "s_mov_b32 s51, 0x20000\n"
      "s_mov_b32 s54, 0x3e000\n"
      "s_mov_b32 s55, 0x20000\n"
      "s_mov_b32 s56, %3\n"
      "v_mbcnt_lo_u32_b32 v208, -1, 0\n"
      "v_mbcnt_hi_u32_b32 v208, -1, v208\n"
      "v_and_b32_e32 v210, 48, v208\n"
      "v_and_b32_e32 v211, 15, v208\n"
      "v_lshl_or_b32 v212, v211, 8, v210\n"
      "s_lshl_b32 s57, s56, 15\n"
      "s_lshl_b32 s68, s56, 6\n"
      "v_add_u32_e32 v249, s68, v212\n"
      "v_add_u32_e32 v250, 0x1000, v249\n"
      "v_bfe_u32 v211, v208, 2, 2\n"
      "v_lshlrev_b32_e32 v211, 8, v211\n"
      "v_lshl_or_b32 v211, v210, 2, v211\n"
      "v_and_b32_e32 v209, 3, v208\n"
      "v_lshl_or_b32 v211, v209, 4, v211\n"
      "v_add_u32_e32 v253, s57, v211\n"
      "v_xor_b32_e32 v255, 32, v253\n"
      "s_lshl_b32 s60, s56, 11\n"
      "s_lshl_b32 s69, s56, 5\n"
      "s_and_b32 s69, s69, 32\n"
      "s_or_b32 s69, s69, s60\n"
      "v_xor_b32_e32 v248, s69, v211\n"
      "s_getpc_b64 s[52:53]\n"
      ".Lpc_%=:\n"
      "s_and_b32 s53, s53, 0xffff\n"
      "s_mov_b32 s54, .Lend_%=-.Lpc_%=\n"
      "v_lshlrev_b32_e32 v214, 7, v208\n"
      "s_lshl_b32 s69, s56, 13\n"
      "v_add_u32_e32 v214, s69, v214\n"
      "buffer_load_dword v232, v214, s[52:55], 0 offen\n"
      "s_mov_b32 s54, 0x3e000\n"
      "s_waitcnt lgkmcnt(0)\n"
      "s_add_u32 s44, s14, s25\n"
      "s_addc_u32 s45, s15, 0\n"
      "s_and_b32 s45, s45, 0xffff\n"
      "buffer_load_dwordx4 v[144:147], v212, s[44:47], s68 offen offset:0\n"
      "s_add_u32 m0, s57, 0\n"
      "s_mov_b32 s69, 0\n"
      "buffer_load_dwordx4 v253, s[44:47], s69 offen offset:0 lds\n"
      "buffer_load_dwordx4 v253, s[44:47], s69 offen offset:1024 lds\n"
      "buffer_load_dwordx4 v255, s[44:47], s69 offen offset:2048 lds\n"
      "buffer_load_dwordx4 v255, s[44:47], s69 offen offset:3072 lds\n"
      "s_add_u32 m0, s57, 0x1000\n"
      "s_mov_b32 s69, 0x1000\n"
      "buffer_load_dwordx4 v253, s[44:47], s69 offen offset:0 lds\n"
      "buffer_load_dwordx4 v253, s[44:47], s69 offen offset:1024 lds\n"
      "buffer_load_dwordx4 v255, s[44:47], s69 offen offset:2048 lds\n"
      "buffer_load_dwordx4 v255, s[44:47], s69 offen offset:3072 lds\n"
      "s_add_u32 s40, s12, s24\n"
      "s_addc_u32 s41, s13, 0\n"
      "s_and_b32 s41, s41, 0xffff\n"
      "s_add_u32 m0, s60, 0x20000\n"
      "s_mov_b32 s58, 0\n"
      "buffer_load_dwordx4 v248, s[40:43], s58 offen offset:0 lds\n"
      "buffer_load_dwordx4 v248, s[40:43], s58 offen offset:1024 lds\n"
      "s_add_u32 s48, s16, s25\n"
      "s_addc_u32 s49, s17, 0\n"
      "s_and_b32 s49, s49, 0xffff\n"
      "s_add_u32 s52, s18, s24\n"
      "s_addc_u32 s53, s19, 0\n"
      "s_and_b32 s53, s53, 0xffff\n"
      "s_add_u32 m0, s57, 0x2000\n"
      "s_mov_b32 s69, 0x2000\n"
      "buffer_load_dwordx4 v253, s[44:47], s69 offen offset:0 lds\n"
      "buffer_load_dwordx4 v253, s[44:47], s69 offen offset:1024 lds\n"
      "s_mov_b32 s62, 0xffff0000\n"
      "s_mov_b32 s63, 0x57800000\n"
      "s_mov_b32 s64, 0x3e0293ee\n"
      "s_mov_b32 s65, 0x3e0293ee\n"
      "s_mov_b32 s70, 0xbc0293ee\n"
      "s_mov_b32 s71, 0xbc0293ee\n"
      "s_mov_b32 s66, 0\n"
      "s_mov_b32 s67, 0x3c000\n"
      "s_mov_b32 s61, 14\n"
      "v_bfe_u32 v211, v208, 2, 2\n"
      "v_lshlrev_b32_e32 v240, 10, v211\n"
      "v_and_b32_e32 v211, 2, v211\n"
      "v_lshlrev_b32_e32 v211, 4, v211\n"
      "v_xor_b32_e32 v211, v211, v210\n"
      "v_or_b32_e32 v240, v240, v211\n"
      "v_lshl_or_b32 v240, v209, 6, v240\n"
      "v_add_u32_e32 v251, s57, v240\n"
      "v_add_u32_e32 v240, 0x20000, v240\n"
      "v_bfe_i32 v241, v208, 0, 1\n"
      "v_and_b32_e32 v241, 0xaaaaaaaa, v241\n"
      "v_xor_b32_e32 v241, 0x44444444, v241\n"
      "v_mov_b32_e32 v224, 0\n"
      "v_mov_b32_e32 v225, 0\n"
      "v_mov_b32_e32 v226, 0\n"
      "v_mov_b32_e32 v227, 0\n"
      "v_mov_b32_e32 v228, 0\n"
      "v_mov_b32_e32 v229, 0\n"
      "v_mov_b32_e32 v230, 0\n"
      "v_mov_b32_e32 v231, 0\n"
      "v_lshlrev_b32_e32 v211, 4, v208\n"
      "v_add_u32_e32 v242, s57, v211\n"
      "v_xor_b32_e32 v243, 0x8000, v242\n"
      "v_xor_b32_e32 v244, 0x10000, v242\n"
      "v_xor_b32_e32 v245, 0x18000, v242\n"
      "v_mul_u32_u24_e32 v236, 48, v210\n"
      "s_lshl_b32 s68, s56, 8\n"
      "v_add3_u32 v236, v236, s68, v242\n"
      "v_xor_b32_e32 v237, 0x8000, v236\n"
      "v_xor_b32_e32 v238, 0x10000, v236\n"
      "v_xor_b32_e32 v239, 0x18000, v236\n"
      "v_add_u32_e32 v247, 0x6000, v211\n"
      "s_lshl_b32 s68, s56, 2\n"
      "v_add_u32_e32 v246, s68, v247\n"
      "v_and_b32_e32 v247, 0xfffffeff, v247\n"
      "v_add_u32_e32 v213, 0x26000, v211\n"
      "v_add_u32_e32 v252, s60, v213\n"
      "v_and_b32_e32 v211, 14, v208\n"
      "v_mov_b32_e32 v209, 0x3f803f80\n"
      "v_cmp_eq_u32_e32 vcc, 0, v211\n"
      "s_nop 1\n"
      "v_cndmask_b32_e32 v216, 0, v209, vcc\n"
      "v_cndmask_b32_e32 v217, 0, v209, vcc\n"
      "v_cndmask_b32_e32 v218, 0, v209, vcc\n"
      "v_cndmask_b32_e32 v219, 0, v209, vcc\n"
      "v_cmp_eq_u32_e32 vcc, 8, v211\n"
      "s_nop 1\n"
      "v_cndmask_b32_e32 v220, 0, v209, vcc\n"
      "v_cndmask_b32_e32 v221, 0, v209, vcc\n"
      "v_cndmask_b32_e32 v222, 0, v209, vcc\n"
      "v_cndmask_b32_e32 v223, 0, v209, vcc\n"
      "s_mov_b64 exec, 0\n"
      "s_cmp_eq_u32 s56, 0\n"
      "s_cbranch_scc1 .Lst0_%=\n"
      "s_cmp_eq_u32 s56, 1\n"
      "s_cbranch_scc1 .Lst1_%=\n"
      "s_cmp_eq_u32 s56, 2\n"
      "s_cbranch_scc1 .Lst2_%=\n"
      "s_branch .Lst3_%=\n"
      ".Lstback_%=:\n"
      "s_mov_b64 exec, -1\n"
      "buffer_load_dwordx4 v255, s[44:47], s69 offen offset:2048 lds\n"
      "buffer_load_dwordx4 v255, s[44:47], s69 offen offset:3072 lds\n"
      "s_waitcnt vmcnt(14)\n"
      "v_lshlrev_b32_e32 v160, 16, v144\n"
      "v_and_b32_e32 v161, s62, v144\n"
      "v_lshlrev_b32_e32 v162, 16, v145\n"
      "v_and_b32_e32 v163, s62, v145\n"
      "v_lshlrev_b32_e32 v164, 16, v146\n"
      "v_and_b32_e32 v165, s62, v146\n"
      "v_lshlrev_b32_e32 v166, 16, v147\n"
      "v_and_b32_e32 v167, s62, v147\n"
      "v_add_f32_dpp v160, v160, v160 row_ror:8 row_mask:0xf bank_mask:0xf\n"
      "v_add_f32_dpp v161, v161, v161 row_ror:8 row_mask:0xf bank_mask:0xf\n"
      "v_add_f32_dpp v162, v162, v162 row_ror:8 row_mask:0xf bank_mask:0xf\n"
      "v_add_f32_dpp v163, v163, v163 row_ror:8 row_mask:0xf bank_mask:0xf\n"
      "v_add_f32_dpp v164, v164, v164 row_ror:8 row_mask:0xf bank_mask:0xf\n"
      "v_add_f32_dpp v165, v165, v165 row_ror:8 row_mask:0xf bank_mask:0xf\n"
      "v_add_f32_dpp v166, v166, v166 row_ror:8 row_mask:0xf bank_mask:0xf\n"
      "v_add_f32_dpp v167, v167, v167 row_ror:8 row_mask:0xf bank_mask:0xf\n"
      "v_add_f32_dpp v160, v160, v160 row_ror:4 row_mask:0xf bank_mask:0xf\n"
      "v_add_f32_dpp v161, v161, v161 row_ror:4 row_mask:0xf bank_mask:0xf\n"
      "v_add_f32_dpp v162, v162, v162 row_ror:4 row_mask:0xf bank_mask:0xf\n"
      "v_add_f32_dpp v163, v163, v163 row_ror:4 row_mask:0xf bank_mask:0xf\n"
      "v_add_f32_dpp v164, v164, v164 row_ror:4 row_mask:0xf bank_mask:0xf\n"
      "v_add_f32_dpp v165, v165, v165 row_ror:4 row_mask:0xf bank_mask:0xf\n"
      "v_add_f32_dpp v166, v166, v166 row_ror:4 row_mask:0xf bank_mask:0xf\n"
      "v_add_f32_dpp v167, v167, v167 row_ror:4 row_mask:0xf bank_mask:0xf\n"
      "v_add_f32_dpp v160, v160, v160 row_ror:2 row_mask:0xf bank_mask:0xf\n"
      "v_add_f32_dpp v161, v161, v161 row_ror:2 row_mask:0xf bank_mask:0xf\n"
      "v_add_f32_dpp v162, v162, v162 row_ror:2 row_mask:0xf bank_mask:0xf\n"
      "v_add_f32_dpp v163, v163, v163 row_ror:2 row_mask:0xf bank_mask:0xf\n"
      "v_add_f32_dpp v164, v164, v164 row_ror:2 row_mask:0xf bank_mask:0xf\n"
      "v_add_f32_dpp v165, v165, v165 row_ror:2 row_mask:0xf bank_mask:0xf\n"
      "v_add_f32_dpp v166, v166, v166 row_ror:2 row_mask:0xf bank_mask:0xf\n"
      "v_add_f32_dpp v167, v167, v167 row_ror:2 row_mask:0xf bank_mask:0xf\n"
      "v_add_f32_dpp v160, v160, v160 row_ror:1 row_mask:0xf bank_mask:0xf\n"
      "v_add_f32_dpp v161, v161, v161 row_ror:1 row_mask:0xf bank_mask:0xf\n"
      "v_add_f32_dpp v162, v162, v162 row_ror:1 row_mask:0xf bank_mask:0xf\n"
      "v_add_f32_dpp v163, v163, v163 row_ror:1 row_mask:0xf bank_mask:0xf\n"
      "v_add_f32_dpp v164, v164, v164 row_ror:1 row_mask:0xf bank_mask:0xf\n"
      "v_add_f32_dpp v165, v165, v165 row_ror:1 row_mask:0xf bank_mask:0xf\n"
      "v_add_f32_dpp v166, v166, v166 row_ror:1 row_mask:0xf bank_mask:0xf\n"
      "v_add_f32_dpp v167, v167, v167 row_ror:1 row_mask:0xf bank_mask:0xf\n"
      "v_pk_mul_f32 v[160:161], v[160:161], s[70:71]\n"
      "v_pk_mul_f32 v[162:163], v[162:163], s[70:71]\n"
      "v_pk_mul_f32 v[164:165], v[164:165], s[70:71]\n"
      "v_pk_mul_f32 v[166:167], v[166:167], s[70:71]\n"
      "ds_write_b128 v252, v[160:163] offset:0\n"
      "ds_write_b128 v252, v[164:167] offset:1024\n"
      "s_add_u32 m0, s57, 0x3000\n"
      "s_mov_b32 s69, 0x3000\n"
      "buffer_load_dwordx4 v253, s[44:47], s69 offen offset:0 lds\n"
      "buffer_load_dwordx4 v253, s[44:47], s69 offen offset:1024 lds\n"
      "buffer_load_dwordx4 v255, s[44:47], s69 offen offset:2048 lds\n"
      "buffer_load_dwordx4 v255, s[44:47], s69 offen offset:3072 lds\n"
      "s_add_u32 m0, s60, 0x22000\n"
      "s_mov_b32 s58, 0x2000\n"
      "buffer_load_dwordx4 v248, s[40:43], s58 offen offset:0 lds\n"
      "buffer_load_dwordx4 v248, s[40:43], s58 offen offset:1024 lds\n"
      "s_waitcnt vmcnt(12)\n"
      "ds_read_b128 v[0:3], v251 offset:0\n"
      "ds_read_b128 v[4:7], v251 offset:256\n"
      "ds_read_b128 v[8:11], v251 offset:512\n"
      "ds_read_b128 v[12:15], v251 offset:768\n"
      "ds_read_b128 v[16:19], v251 offset:4096\n"
      "ds_read_b128 v[20:23], v251 offset:4352\n"
      "ds_read_b128 v[24:27], v251 offset:4608\n"
      "ds_read_b128 v[28:31], v251 offset:4864\n"
      "s_waitcnt vmcnt(10) lgkmcnt(8)\n"
      "s_barrier\n"
      "ds_read_b128 v[176:179], v213 offset:0\n"
      "ds_read_b128 v[180:183], v213 offset:1024\n"
      "ds_read_b128 v[184:187], v213 offset:2048\n"
      "ds_read_b128 v[188:191], v213 offset:3072\n"
      "ds_read_b128 v[192:195], v213 offset:4096\n"
      "ds_read_b128 v[196:199], v213 offset:5120\n"
      "ds_read_b128 v[200:203], v213 offset:6144\n"
      "ds_read_b128 v[204:207], v213 offset:7168\n"
      "ds_read_b128 a[128:131], v240 offset:0\n"
      "ds_read_b128 a[132:135], v240 offset:256\n"
      "ds_read_b128 a[136:139], v240 offset:512\n"
      "ds_read_b128 a[140:143], v240 offset:768\n"
      "ds_read_b128 a[144:147], v240 offset:4096\n"
      "ds_read_b128 a[148:151], v240 offset:4352\n"
      "ds_read_b128 a[152:155], v240 offset:4608\n"
      "ds_read_b128 a[156:159], v240 offset:4864\n"
      "s_waitcnt lgkmcnt(8)\n"
      "v_lshlrev_b32_e32 v208, 16, v0\n"
      "v_lshlrev_b32_e32 v210, 16, v1\n"
      "v_lshlrev_b32_e32 v212, 16, v2\n"
      "v_lshlrev_b32_e32 v214, 16, v3\n"
      "s_add_u32 m0, s57, 0x4000\n"
      "s_mov_b32 s69, 0x4000\n"
      "buffer_load_dwordx4 v253, s[44:47], s69 offen offset:0 lds\n"
      "v_and_b32_e32 v209, s62, v0\n"
      "v_and_b32_e32 v211, s62, v1\n"
      "v_and_b32_e32 v213, s62, v2\n"
      "v_and_b32_e32 v215, s62, v3\n"
      "v_pk_fma_f32 v[208:209], v[208:209], s[64:65], v[176:177]\n"
      "v_pk_fma_f32 v[210:211], v[210:211], s[64:65], v[178:179]\n"
      "v_pk_fma_f32 v[212:213], v[212:213], s[64:65], v[180:181]\n"
      "v_pk_fma_f32 v[214:215], v[214:215], s[64:65], v[182:183]\n"
      "v_cvt_pk_bf16_f32 v0, v208, v209\n"
      "v_cvt_pk_bf16_f32 v1, v210, v211\n"
      "v_cvt_pk_bf16_f32 v2, v212, v213\n"
      "v_cvt_pk_bf16_f32 v3, v214, v215\n"
      "v_lshlrev_b32_e32 v208, 16, v4\n"
      "v_lshlrev_b32_e32 v210, 16, v5\n"
      "v_lshlrev_b32_e32 v212, 16, v6\n"
      "v_lshlrev_b32_e32 v214, 16, v7\n"
      "v_and_b32_e32 v209, s62, v4\n"
      "v_and_b32_e32 v211, s62, v5\n"
      "v_and_b32_e32 v213, s62, v6\n"
      "v_and_b32_e32 v215, s62, v7\n"
      "v_pk_fma_f32 v[208:209], v[208:209], s[64:65], v[184:185]\n"
      "v_pk_fma_f32 v[210:211], v[210:211], s[64:65], v[186:187]\n"
      "v_pk_fma_f32 v[212:213], v[212:213], s[64:65], v[188:189]\n"
      "v_pk_fma_f32 v[214:215], v[214:215], s[64:65], v[190:191]\n"
      "v_cvt_pk_bf16_f32 v4, v208, v209\n"
      "v_cvt_pk_bf16_f32 v5, v210, v211\n"
      "v_cvt_pk_bf16_f32 v6, v212, v213\n"
      "v_cvt_pk_bf16_f32 v7, v214, v215\n"
      "buffer_load_dwordx4 v253, s[44:47], s69 offen offset:1024 lds\n"
      "v_lshlrev_b32_e32 v208, 16, v8\n"
      "v_lshlrev_b32_e32 v210, 16, v9\n"
      "v_lshlrev_b32_e32 v212, 16, v10\n"
      "v_lshlrev_b32_e32 v214, 16, v11\n"
      "v_and_b32_e32 v209, s62, v8\n"
      "v_and_b32_e32 v211, s62, v9\n"
      "v_and_b32_e32 v213, s62, v10\n"
      "v_and_b32_e32 v215, s62, v11\n"
      "v_pk_fma_f32 v[208:209], v[208:209], s[64:65], v[192:193]\n"
      "v_pk_fma_f32 v[210:211], v[210:211], s[64:65], v[194:195]\n"
      "v_pk_fma_f32 v[212:213], v[212:213], s[64:65], v[196:197]\n"
      "v_pk_fma_f32 v[214:215], v[214:215], s[64:65], v[198:199]\n"
      "v_cvt_pk_bf16_f32 v8, v208, v209\n"
      "v_cvt_pk_bf16_f32 v9, v210, v211\n"
      "v_cvt_pk_bf16_f32 v10, v212, v213\n"
      "v_cvt_pk_bf16_f32 v11, v214, v215\n"
      "v_lshlrev_b32_e32 v208, 16, v12\n"
      "v_lshlrev_b32_e32 v210, 16, v13\n"
      "v_lshlrev_b32_e32 v212, 16, v14\n"
      "v_lshlrev_b32_e32 v214, 16, v15\n"
      "v_and_b32_e32 v209, s62, v12\n"
      "v_and_b32_e32 v211, s62, v13\n"
      "v_and_b32_e32 v213, s62, v14\n"
      "v_and_b32_e32 v215, s62, v15\n"
      "v_pk_fma_f32 v[208:209], v[208:209], s[64:65], v[200:201]\n"
      "v_pk_fma_f32 v[210:211], v[210:211], s[64:65], v[202:203]\n"
      "v_pk_fma_f32 v[212:213], v[212:213], s[64:65], v[204:205]\n"
      "buffer_load_dwordx4 v255, s[44:47], s69 offen offset:2048 lds\n"
      "v_pk_fma_f32 v[214:215], v[214:215], s[64:65], v[206:207]\n"
      "v_cvt_pk_bf16_f32 v12, v208, v209\n"
      "v_cvt_pk_bf16_f32 v13, v210, v211\n"
      "v_cvt_pk_bf16_f32 v14, v212, v213\n"
      "v_cvt_pk_bf16_f32 v15, v214, v215\n"
      "v_lshlrev_b32_e32 v208, 16, v16\n"
      "v_lshlrev_b32_e32 v210, 16, v17\n"
      "v_lshlrev_b32_e32 v212, 16, v18\n"
      "v_lshlrev_b32_e32 v214, 16, v19\n"
      "v_and_b32_e32 v209, s62, v16\n"
      "v_and_b32_e32 v211, s62, v17\n"
      "v_and_b32_e32 v213, s62, v18\n"
      "v_and_b32_e32 v215, s62, v19\n"
      "v_pk_fma_f32 v[208:209], v[208:209], s[64:65], v[176:177]\n"
      "v_pk_fma_f32 v[210:211], v[210:211], s[64:65], v[178:179]\n"
      "v_pk_fma_f32 v[212:213], v[212:213], s[64:65], v[180:181]\n"
      "v_pk_fma_f32 v[214:215], v[214:215], s[64:65], v[182:183]\n"
      "v_cvt_pk_bf16_f32 v16, v208, v209\n"
      "v_cvt_pk_bf16_f32 v17, v210, v211\n"
      "v_cvt_pk_bf16_f32 v18, v212, v213\n"
      "v_cvt_pk_bf16_f32 v19, v214, v215\n"
      "v_lshlrev_b32_e32 v208, 16, v20\n"
      "v_lshlrev_b32_e32 v210, 16, v21\n"
      "v_lshlrev_b32_e32 v212, 16, v22\n"
      "v_lshlrev_b32_e32 v214, 16, v23\n"
      "v_and_b32_e32 v209, s62, v20\n"
      "v_and_b32_e32 v211, s62, v21\n"
      "v_and_b32_e32 v213, s62, v22\n"
      "buffer_load_dwordx4 v255, s[44:47], s69 offen offset:3072 lds\n"
      "v_and_b32_e32 v215, s62, v23\n"
      "v_pk_fma_f32 v[208:209], v[208:209], s[64:65], v[184:185]\n"
      "v_pk_fma_f32 v[210:211], v[210:211], s[64:65], v[186:187]\n"
      "v_pk_fma_f32 v[212:213], v[212:213], s[64:65], v[188:189]\n"
      "v_pk_fma_f32 v[214:215], v[214:215], s[64:65], v[190:191]\n"
      "v_cvt_pk_bf16_f32 v20, v208, v209\n"
      "v_cvt_pk_bf16_f32 v21, v210, v211\n"
      "v_cvt_pk_bf16_f32 v22, v212, v213\n"
      "v_cvt_pk_bf16_f32 v23, v214, v215\n"
      "v_lshlrev_b32_e32 v208, 16, v24\n"
      "v_lshlrev_b32_e32 v210, 16, v25\n"
      "v_lshlrev_b32_e32 v212, 16, v26\n"
      "v_lshlrev_b32_e32 v214, 16, v27\n"
      "v_and_b32_e32 v209, s62, v24\n"
      "v_and_b32_e32 v211, s62, v25\n"
      "v_and_b32_e32 v213, s62, v26\n"
      "v_and_b32_e32 v215, s62, v27\n"
      "v_pk_fma_f32 v[208:209], v[208:209], s[64:65], v[192:193]\n"
      "v_pk_fma_f32 v[210:211], v[210:211], s[64:65], v[194:195]\n"
      "v_pk_fma_f32 v[212:213], v[212:213], s[64:65], v[196:197]\n"
      "v_pk_fma_f32 v[214:215], v[214:215], s[64:65], v[198:199]\n"
      "v_cvt_pk_bf16_f32 v24, v208, v209\n"
      "v_cvt_pk_bf16_f32 v25, v210, v211\n"
      "v_cvt_pk_bf16_f32 v26, v212, v213\n"
      "v_cvt_pk_bf16_f32 v27, v214, v215\n"
      "v_lshlrev_b32_e32 v208, 16, v28\n"
      "v_lshlrev_b32_e32 v210, 16, v29\n"
      "s_add_u32 m0, s57, 0x5000\n"
      "s_mov_b32 s69, 0x5000\n"
      "buffer_load_dwordx4 v253, s[44:47], s69 offen offset:0 lds\n"
      "v_lshlrev_b32_e32 v212, 16, v30\n"
      "v_lshlrev_b32_e32 v214, 16, v31\n"
      "v_and_b32_e32 v209, s62, v28\n"
      "v_and_b32_e32 v211, s62, v29\n"
      "v_and_b32_e32 v213, s62, v30\n"
      "v_and_b32_e32 v215, s62, v31\n"
      "v_pk_fma_f32 v[208:209], v[208:209], s[64:65], v[200:201]\n"
      "v_pk_fma_f32 v[210:211], v[210:211], s[64:65], v[202:203]\n"
      "v_pk_fma_f32 v[212:213], v[212:213], s[64:65], v[204:205]\n"
      "v_pk_fma_f32 v[214:215], v[214:215], s[64:65], v[206:207]\n"
      "v_cvt_pk_bf16_f32 v28, v208, v209\n"
      "v_cvt_pk_bf16_f32 v29, v210, v211\n"
      "v_cvt_pk_bf16_f32 v30, v212, v213\n"
      "v_cvt_pk_bf16_f32 v31, v214, v215\n"
      "s_waitcnt lgkmcnt(0)\n"
      "v_mfma_f32_16x16x32_bf16 v[128:131], v[0:3], a[128:131], 0\n"
      "v_mfma_f32_16x16x32_bf16 v[128:131], v[4:7], a[132:135], v[128:131]\n"
      "v_mfma_f32_16x16x32_bf16 v[128:131], v[8:11], a[136:139], v[128:131]\n"
      "v_mfma_f32_16x16x32_bf16 v[128:131], v[12:15], a[140:143], v[128:131]\n"
      "buffer_load_dwordx4 v253, s[44:47], s69 offen offset:1024 lds\n"
      "v_mfma_f32_16x16x32_bf16 v[132:135], v[28:31], a[140:143], 0\n"
      "v_mfma_f32_16x16x32_bf16 v[132:135], v[24:27], a[136:139], v[132:135]\n"
      "v_mfma_f32_16x16x32_bf16 v[132:135], v[20:23], a[132:135], v[132:135]\n"
      "v_mfma_f32_16x16x32_bf16 v[132:135], v[16:19], a[128:131], v[132:135]\n"
      "s_nop 2\n"
      "v_exp_f32_e32 v128, v128\n"
      "v_exp_f32_e32 v129, v129\n"
      "v_exp_f32_e32 v130, v130\n"
      "v_exp_f32_e32 v131, v131\n"
      "s_nop 0\n"
      "v_exp_f32_e32 v132, v132\n"
      "v_mfma_f32_16x16x32_bf16 v[136:139], v[0:3], a[144:147], 0\n"
      "v_cvt_pk_bf16_f32 v144, v128, v129\n"
      "v_mfma_f32_16x16x32_bf16 v[136:139], v[4:7], a[148:151], v[136:139]\n"
      "v_exp_f32_e32 v133, v133\n"
      "v_mfma_f32_16x16x32_bf16 v[136:139], v[8:11], a[152:155], v[136:139]\n"
      "buffer_load_dwordx4 v255, s[44:47], s69 offen offset:2048 lds\n"
      "v_cvt_pk_bf16_f32 v145, v130, v131\n"
      "v_mfma_f32_16x16x32_bf16 v[136:139], v[12:15], a[156:159], v[136:139]\n"
      "v_exp_f32_e32 v134, v134\n"
      "v_mfma_f32_16x16x32_bf16 v[140:143], v[28:31], a[156:159], 0\n"
      "v_cvt_pk_bf16_f32 v146, v132, v133\n"
      "v_mfma_f32_16x16x32_bf16 v[140:143], v[24:27], a[152:155], v[140:143]\n"
      "v_exp_f32_e32 v135, v135\n"
      "v_mfma_f32_16x16x32_bf16 v[140:143], v[20:23], a[148:151], v[140:143]\n"
      "v_cvt_pk_bf16_f32 v147, v134, v135\n"
      "v_mfma_f32_16x16x32_bf16 v[140:143], v[16:19], a[144:147], v[140:143]\n"
      "v_exp_f32_e32 v136, v136\n"
      "s_waitcnt vmcnt(9)\n"
      "ds_read_b128 v[32:35], v251 offset:8192\n"
      "ds_read_b128 v[36:39], v251 offset:8448\n"
      "ds_read_b128 v[40:43], v251 offset:8704\n"
      "ds_read_b128 v[44:47], v251 offset:8960\n"
      "ds_read_b128 v[48:51], v251 offset:12288\n"
      "buffer_load_dwordx4 v255, s[44:47], s69 offen offset:3072 lds\n"
      "ds_read_b128 v[52:55], v251 offset:12544\n"
      "ds_read_b128 v[56:59], v251 offset:12800\n"
      "ds_read_b128 v[60:63], v251 offset:13056\n"
      "ds_write_b128 v242, v[144:147] offset:0\n"
      "v_exp_f32_e32 v137, v137\n"
      "v_exp_f32_e32 v138, v138\n"
      "v_exp_f32_e32 v139, v139\n"
      "v_exp_f32_e32 v140, v140\n"
      "s_waitcnt lgkmcnt(1)\n"
      "v_lshlrev_b32_e32 v208, 16, v32\n"
      "v_lshlrev_b32_e32 v210, 16, v33\n"
      "v_lshlrev_b32_e32 v212, 16, v34\n"
      "v_lshlrev_b32_e32 v214, 16, v35\n"
      "v_and_b32_e32 v209, s62, v32\n"
      "s_add_u32 m0, s57, 0x6000\n"
      "s_mov_b32 s69, 0x6000\n"
      "buffer_load_dwordx4 v253, s[44:47], s69 offen offset:0 lds\n"
      "v_and_b32_e32 v211, s62, v33\n"
      "v_and_b32_e32 v213, s62, v34\n"
      "v_and_b32_e32 v215, s62, v35\n"
      "v_pk_fma_f32 v[208:209], v[208:209], s[64:65], v[176:177]\n"
      "v_pk_fma_f32 v[210:211], v[210:211], s[64:65], v[178:179]\n"
      "v_pk_fma_f32 v[212:213], v[212:213], s[64:65], v[180:181]\n"
      "v_pk_fma_f32 v[214:215], v[214:215], s[64:65], v[182:183]\n"
      "v_cvt_pk_bf16_f32 v32, v208, v209\n"
      "v_cvt_pk_bf16_f32 v33, v210, v211\n"
      "v_cvt_pk_bf16_f32 v34, v212, v213\n"
      "v_cvt_pk_bf16_f32 v35, v214, v215\n"
      "v_lshlrev_b32_e32 v208, 16, v36\n"
      "v_lshlrev_b32_e32 v210, 16, v37\n"
      "v_lshlrev_b32_e32 v212, 16, v38\n"
      "v_lshlrev_b32_e32 v214, 16, v39\n"
      "v_and_b32_e32 v209, s62, v36\n"
      "v_and_b32_e32 v211, s62, v37\n"
      "v_and_b32_e32 v213, s62, v38\n"
      "v_and_b32_e32 v215, s62, v39\n"
      "v_pk_fma_f32 v[208:209], v[208:209], s[64:65], v[184:185]\n"
      "v_pk_fma_f32 v[210:211], v[210:211], s[64:65], v[186:187]\n"
      "v_pk_fma_f32 v[212:213], v[212:213], s[64:65], v[188:189]\n"
      "v_pk_fma_f32 v[214:215], v[214:215], s[64:65], v[190:191]\n"
      "v_cvt_pk_bf16_f32 v36, v208, v209\n"
      "v_cvt_pk_bf16_f32 v37, v210, v211\n"
      "v_cvt_pk_bf16_f32 v38, v212, v213\n"
      "v_cvt_pk_bf16_f32 v39, v214, v215\n"
      "v_lshlrev_b32_e32 v208, 16, v40\n"
      "buffer_load_dwordx4 v253, s[44:47], s69 offen offset:1024 lds\n"
      "v_lshlrev_b32_e32 v210, 16, v41\n"
      "v_lshlrev_b32_e32 v212, 16, v42\n"
      "v_lshlrev_b32_e32 v214, 16, v43\n"
      "v_and_b32_e32 v209, s62, v40\n"
      "v_and_b32_e32 v211, s62, v41\n"
      "v_and_b32_e32 v213, s62, v42\n"
      "v_and_b32_e32 v215, s62, v43\n"
      "v_pk_fma_f32 v[208:209], v[208:209], s[64:65], v[192:193]\n"
      "v_pk_fma_f32 v[210:211], v[210:211], s[64:65], v[194:195]\n"
      "v_pk_fma_f32 v[212:213], v[212:213], s[64:65], v[196:197]\n"
      "v_pk_fma_f32 v[214:215], v[214:215], s[64:65], v[198:199]\n"
      "v_cvt_pk_bf16_f32 v40, v208, v209\n"
      "v_cvt_pk_bf16_f32 v41, v210, v211\n"
      "v_cvt_pk_bf16_f32 v42, v212, v213\n"
      "v_cvt_pk_bf16_f32 v43, v214, v215\n"
      "v_lshlrev_b32_e32 v208, 16, v44\n"
      "v_lshlrev_b32_e32 v210, 16, v45\n"
      "v_lshlrev_b32_e32 v212, 16, v46\n"
      "v_lshlrev_b32_e32 v214, 16, v47\n"
      "v_and_b32_e32 v209, s62, v44\n"
      "v_and_b32_e32 v211, s62, v45\n"
      "v_and_b32_e32 v213, s62, v46\n"
      "v_and_b32_e32 v215, s62, v47\n"
      "v_pk_fma_f32 v[208:209], v[208:209], s[64:65], v[200:201]\n"
      "v_pk_fma_f32 v[210:211], v[210:211], s[64:65], v[202:203]\n"
      "v_pk_fma_f32 v[212:213], v[212:213], s[64:65], v[204:205]\n"
      "v_pk_fma_f32 v[214:215], v[214:215], s[64:65], v[206:207]\n"
      "buffer_load_dwordx4 v255, s[44:47], s69 offen offset:2048 lds\n"
      "v_cvt_pk_bf16_f32 v44, v208, v209\n"
      "v_cvt_pk_bf16_f32 v45, v210, v211\n"
      "v_cvt_pk_bf16_f32 v46, v212, v213\n"
      "v_cvt_pk_bf16_f32 v47, v214, v215\n"
      "v_lshlrev_b32_e32 v208, 16, v48\n"
      "v_lshlrev_b32_e32 v210, 16, v49\n"
      "v_lshlrev_b32_e32 v212, 16, v50\n"
      "v_lshlrev_b32_e32 v214, 16, v51\n"
      "v_and_b32_e32 v209, s62, v48\n"
      "v_and_b32_e32 v211, s62, v49\n"
      "v_and_b32_e32 v213, s62, v50\n"
      "v_and_b32_e32 v215, s62, v51\n"
      "v_pk_fma_f32 v[208:209], v[208:209], s[64:65], v[176:177]\n"
      "v_pk_fma_f32 v[210:211], v[210:211], s[64:65], v[178:179]\n"
      "v_pk_fma_f32 v[212:213], v[212:213], s[64:65], v[180:181]\n"
      "v_pk_fma_f32 v[214:215], v[214:215], s[64:65], v[182:183]\n"
      "v_cvt_pk_bf16_f32 v48, v208, v209\n"
      "v_cvt_pk_bf16_f32 v49, v210, v211\n"
      "v_cvt_pk_bf16_f32 v50, v212, v213\n"
      "v_cvt_pk_bf16_f32 v51, v214, v215\n"
      "v_lshlrev_b32_e32 v208, 16, v52\n"
      "v_lshlrev_b32_e32 v210, 16, v53\n"
      "v_lshlrev_b32_e32 v212, 16, v54\n"
      "v_lshlrev_b32_e32 v214, 16, v55\n"
      "v_and_b32_e32 v209, s62, v52\n"
      "v_and_b32_e32 v211, s62, v53\n"
      "v_and_b32_e32 v213, s62, v54\n"
      "v_and_b32_e32 v215, s62, v55\n"
      "buffer_load_dwordx4 v255, s[44:47], s69 offen offset:3072 lds\n"
      "v_pk_fma_f32 v[208:209], v[208:209], s[64:65], v[184:185]\n"
      "v_pk_fma_f32 v[210:211], v[210:211], s[64:65], v[186:187]\n"
      "v_pk_fma_f32 v[212:213], v[212:213], s[64:65], v[188:189]\n"
      "v_pk_fma_f32 v[214:215], v[214:215], s[64:65], v[190:191]\n"
      "v_cvt_pk_bf16_f32 v52, v208, v209\n"
      "v_cvt_pk_bf16_f32 v53, v210, v211\n"
      "v_cvt_pk_bf16_f32 v54, v212, v213\n"
      "v_cvt_pk_bf16_f32 v55, v214, v215\n"
      "v_lshlrev_b32_e32 v208, 16, v56\n"
      "v_lshlrev_b32_e32 v210, 16, v57\n"
      "v_lshlrev_b32_e32 v212, 16, v58\n"
      "v_lshlrev_b32_e32 v214, 16, v59\n"
      "v_and_b32_e32 v209, s62, v56\n"
      "v_and_b32_e32 v211, s62, v57\n"
      "v_and_b32_e32 v213, s62, v58\n"
      "v_and_b32_e32 v215, s62, v59\n"
      "v_pk_fma_f32 v[208:209], v[208:209], s[64:65], v[192:193]\n"
      "v_pk_fma_f32 v[210:211], v[210:211], s[64:65], v[194:195]\n"
      "v_pk_fma_f32 v[212:213], v[212:213], s[64:65], v[196:197]\n"
      "v_pk_fma_f32 v[214:215], v[214:215], s[64:65], v[198:199]\n"
      "v_cvt_pk_bf16_f32 v56, v208, v209\n"
      "v_cvt_pk_bf16_f32 v57, v210, v211\n"
      "v_cvt_pk_bf16_f32 v58, v212, v213\n"
      "v_cvt_pk_bf16_f32 v59, v214, v215\n"
      "v_lshlrev_b32_e32 v208, 16, v60\n"
      "v_lshlrev_b32_e32 v210, 16, v61\n"
      "v_lshlrev_b32_e32 v212, 16, v62\n"
      "s_add_u32 m0, s57, 0x7000\n"
      "s_mov_b32 s69, 0x7000\n"
      "buffer_load_dwordx4 v253, s[44:47], s69 offen offset:0 lds\n"
      "v_lshlrev_b32_e32 v214, 16, v63\n"
      "v_and_b32_e32 v209, s62, v60\n"
      "v_and_b32_e32 v211, s62, v61\n"
      "v_and_b32_e32 v213, s62, v62\n"
      "v_and_b32_e32 v215, s62, v63\n"
      "v_pk_fma_f32 v[208:209], v[208:209], s[64:65], v[200:201]\n"
      "v_pk_fma_f32 v[210:211], v[210:211], s[64:65], v[202:203]\n"
      "v_pk_fma_f32 v[212:213], v[212:213], s[64:65], v[204:205]\n"
      "v_pk_fma_f32 v[214:215], v[214:215], s[64:65], v[206:207]\n"
      "v_cvt_pk_bf16_f32 v60, v208, v209\n"
      "v_cvt_pk_bf16_f32 v61, v210, v211\n"
      "v_cvt_pk_bf16_f32 v62, v212, v213\n"
      "v_cvt_pk_bf16_f32 v63, v214, v215\n"
      "v_mfma_f32_16x16x32_bf16 v[128:131], v[32:35], a[128:131], 0\n"
      "v_cvt_pk_bf16_f32 v160, v136, v137\n"
      "v_mfma_f32_16x16x32_bf16 v[128:131], v[36:39], a[132:135], v[128:131]\n"
      "v_exp_f32_e32 v141, v141\n"
      "v_mfma_f32_16x16x32_bf16 v[128:131], v[40:43], a[136:139], v[128:131]\n"
      "v_cvt_pk_bf16_f32 v161, v138, v139\n"
      "v_mfma_f32_16x16x32_bf16 v[128:131], v[44:47], a[140:143], v[128:131]\n"
      "buffer_load_dwordx4 v253, s[44:47], s69 offen offset:1024 lds\n"
      "v_exp_f32_e32 v142, v142\n"
      "v_mfma_f32_16x16x32_bf16 v[132:135], v[60:63], a[140:143], 0\n"
      "v_cvt_pk_bf16_f32 v162, v140, v141\n"
      "v_mfma_f32_16x16x32_bf16 v[132:135], v[56:59], a[136:139], v[132:135]\n"
      "v_exp_f32_e32 v143, v143\n"
      "v_mfma_f32_16x16x32_bf16 v[132:135], v[52:55], a[132:135], v[132:135]\n"
      "v_cvt_pk_bf16_f32 v163, v142, v143\n"
      "v_mfma_f32_16x16x32_bf16 v[132:135], v[48:51], a[128:131], v[132:135]\n"
      "v_exp_f32_e32 v128, v128\n"
      "ds_write_b128 v242, v[160:163] offset:1024\n"
      "v_exp_f32_e32 v129, v129\n"
      "v_exp_f32_e32 v130, v130\n"
      "v_exp_f32_e32 v131, v131\n"
      "buffer_load_dwordx4 v255, s[44:47], s69 offen offset:2048 lds\n"
      "s_waitcnt vmcnt(15) lgkmcnt(0)\n"
      "s_barrier\n"
      "v_exp_f32_e32 v132, v132\n"
      "v_mfma_f32_16x16x32_bf16 v[136:139], v[32:35], a[144:147], 0\n"
      "ds_read_b128 a[192:195], v240 offset:8192\n"
      "v_cvt_pk_bf16_f32 v148, v128, v129\n"
      "v_mfma_f32_16x16x32_bf16 v[136:139], v[36:39], a[148:151], v[136:139]\n"
      "ds_read_b128 a[196:199], v240 offset:8448\n"
      "v_exp_f32_e32 v133, v133\n"
      "v_mfma_f32_16x16x32_bf16 v[136:139], v[40:43], a[152:155], v[136:139]\n"
      "buffer_load_dwordx4 v255, s[44:47], s69 offen offset:3072 lds\n"
      "ds_read_b128 a[200:203], v240 offset:8704\n"
      "v_cvt_pk_bf16_f32 v149, v130, v131\n"
      "v_mfma_f32_16x16x32_bf16 v[136:139], v[44:47], a[156:159], v[136:139]\n"
      "ds_read_b128 a[204:207], v240 offset:8960\n"
      "v_exp_f32_e32 v134, v134\n"
      "v_mfma_f32_16x16x32_bf16 v[140:143], v[60:63], a[156:159], 0\n"
      "ds_read_b128 a[208:211], v240 offset:12288\n"
      "v_cvt_pk_bf16_f32 v150, v132, v133\n"
      "v_mfma_f32_16x16x32_bf16 v[140:143], v[56:59], a[152:155], v[140:143]\n"
      "ds_read_b128 a[212:215], v240 offset:12544\n"
      "v_exp_f32_e32 v135, v135\n"
      "v_mfma_f32_16x16x32_bf16 v[140:143], v[52:55], a[148:151], v[140:143]\n"
      "ds_read_b128 a[216:219], v240 offset:12800\n"
      "v_cvt_pk_bf16_f32 v151, v134, v135\n"
      "v_mfma_f32_16x16x32_bf16 v[140:143], v[48:51], a[144:147], v[140:143]\n"
      "s_add_u32 m0, s57, 0x2000\n"
      "s_mov_b32 s69, 0\n"
      "buffer_load_dwordx4 v253, s[48:51], s69 offen offset:0 lds\n"
      "ds_read_b128 a[220:223], v240 offset:13056\n"
      "v_exp_f32_e32 v136, v136\n"
      "s_waitcnt vmcnt(9)\n"
      "ds_read_b128 v[64:67], v251 offset:16384\n"
      "ds_read_b128 v[68:71], v251 offset:16640\n"
      "ds_read_b128 v[72:75], v251 offset:16896\n"
      "ds_read_b128 v[76:79], v251 offset:17152\n"
      "ds_read_b128 v[80:83], v251 offset:20480\n"
      "ds_read_b128 v[84:87], v251 offset:20736\n"
      "ds_read_b128 v[88:91], v251 offset:20992\n"
      "ds_read_b128 v[92:95], v251 offset:21248\n"
      "v_smfmac_f32_16x16x64_bf16 v[224:227], v[216:219], v[144:151], v241\n"
      "ds_write_b128 v242, v[148:151] offset:2048\n"
      "buffer_load_dwordx4 v253, s[48:51], s69 offen offset:1024 lds\n"
      "v_exp_f32_e32 v137, v137\n"
      "v_exp_f32_e32 v138, v138\n"
      "v_exp_f32_e32 v139, v139\n"
      "s_waitcnt lgkmcnt(1)\n"
      "s_waitcnt lgkmcnt(9)\n"
      "v_exp_f32_e32 v140, v140\n"
      "v_mfma_f32_16x16x32_bf16 v[128:131], v[0:3], a[192:195], 0\n"
      "v_cvt_pk_bf16_f32 v164, v136, v137\n"
      "v_mfma_f32_16x16x32_bf16 v[128:131], v[4:7], a[196:199], v[128:131]\n"
      "v_exp_f32_e32 v141, v141\n"
      "v_mfma_f32_16x16x32_bf16 v[128:131], v[8:11], a[200:203], v[128:131]\n"
      "v_cvt_pk_bf16_f32 v165, v138, v139\n"
      "v_mfma_f32_16x16x32_bf16 v[128:131], v[12:15], a[204:207], v[128:131]\n"
      "buffer_load_dwordx4 v253, s[48:51], s69 offen offset:2048 lds\n"
      "v_exp_f32_e32 v142, v142\n"
      "v_mfma_f32_16x16x32_bf16 v[132:135], v[28:31], a[204:207], 0\n"
      "v_cvt_pk_bf16_f32 v166, v140, v141\n"
      "v_mfma_f32_16x16x32_bf16 v[132:135], v[24:27], a[200:203], v[132:135]\n"
      ".Lst0_%=:\n"
      "s_cbranch_execz .Lst4_%=\n"
      "v_exp_f32_e32 v143, v143\n"
      "v_mfma_f32_16x16x32_bf16 v[132:135], v[20:23], a[196:199], v[132:135]\n"
      "v_cvt_pk_bf16_f32 v167, v142, v143\n"
      "v_mfma_f32_16x16x32_bf16 v[132:135], v[16:19], a[192:195], v[132:135]\n"
      "v_exp_f32_e32 v128, v128\n"
      "v_smfmac_f32_16x16x64_bf16 v[224:227], v[220:223], v[160:167], v241\n"
      "ds_write_b128 v242, v[164:167] offset:3072\n"
      "v_exp_f32_e32 v129, v129\n"
      "v_exp_f32_e32 v130, v130\n"
      "v_exp_f32_e32 v131, v131\n"
      "buffer_load_dwordx4 v253, s[48:51], s69 offen offset:3072 lds\n"
      "s_nop 0\n"
      "v_exp_f32_e32 v132, v132\n"
      "v_mfma_f32_16x16x32_bf16 v[136:139], v[0:3], a[208:211], 0\n"
      "v_cvt_pk_bf16_f32 v152, v128, v129\n"
      "v_mfma_f32_16x16x32_bf16 v[136:139], v[4:7], a[212:215], v[136:139]\n"
      "v_exp_f32_e32 v133, v133\n"
      "v_mfma_f32_16x16x32_bf16 v[136:139], v[8:11], a[216:219], v[136:139]\n"
      "v_cvt_pk_bf16_f32 v153, v130, v131\n"
      "v_mfma_f32_16x16x32_bf16 v[136:139], v[12:15], a[220:223], v[136:139]\n"
      "v_exp_f32_e32 v134, v134\n"
      "v_mfma_f32_16x16x32_bf16 v[140:143], v[28:31], a[220:223], 0\n"
      "v_cvt_pk_bf16_f32 v154, v132, v133\n"
      "v_mfma_f32_16x16x32_bf16 v[140:143], v[24:27], a[216:219], v[140:143]\n"
      "s_add_u32 m0, s57, 0x3000\n"
      "s_mov_b32 s69, 0x1000\n"
      "buffer_load_dwordx4 v253, s[48:51], s69 offen offset:0 lds\n"
      "v_exp_f32_e32 v135, v135\n"
      "v_mfma_f32_16x16x32_bf16 v[140:143], v[20:23], a[212:215], v[140:143]\n"
      "v_cvt_pk_bf16_f32 v155, v134, v135\n"
      "v_mfma_f32_16x16x32_bf16 v[140:143], v[16:19], a[208:211], v[140:143]\n"
      "v_exp_f32_e32 v136, v136\n"
      "ds_write_b128 v242, v[152:155] offset:4096\n"
      "v_exp_f32_e32 v137, v137\n"
      "v_exp_f32_e32 v138, v138\n"
      "v_exp_f32_e32 v139, v139\n"
      "s_nop 2\n"
      "v_exp_f32_e32 v140, v140\n"
      "v_lshlrev_b32_e32 v208, 16, v64\n"
      "v_lshlrev_b32_e32 v210, 16, v65\n"
      "v_lshlrev_b32_e32 v212, 16, v66\n"
      "v_lshlrev_b32_e32 v214, 16, v67\n"
      "v_and_b32_e32 v209, s62, v64\n"
      "v_and_b32_e32 v211, s62, v65\n"
      "v_and_b32_e32 v213, s62, v66\n"
      "v_and_b32_e32 v215, s62, v67\n"
      "buffer_load_dwordx4 v253, s[48:51], s69 offen offset:1024 lds\n"
      "v_pk_fma_f32 v[208:209], v[208:209], s[64:65], v[176:177]\n"
      "v_pk_fma_f32 v[210:211], v[210:211], s[64:65], v[178:179]\n"
      "v_pk_fma_f32 v[212:213], v[212:213], s[64:65], v[180:181]\n"
      "v_pk_fma_f32 v[214:215], v[214:215], s[64:65], v[182:183]\n"
      "v_cvt_pk_bf16_f32 v64, v208, v209\n"
      "v_cvt_pk_bf16_f32 v65, v210, v211\n"
      "v_cvt_pk_bf16_f32 v66, v212, v213\n"
      "v_cvt_pk_bf16_f32 v67, v214, v215\n"
      "v_lshlrev_b32_e32 v208, 16, v68\n"
      "v_lshlrev_b32_e32 v210, 16, v69\n"
      "v_lshlrev_b32_e32 v212, 16, v70\n"
      "v_lshlrev_b32_e32 v214, 16, v71\n"
      "v_and_b32_e32 v209, s62, v68\n"
      "v_and_b32_e32 v211, s62, v69\n"
      "v_and_b32_e32 v213, s62, v70\n"
      "v_and_b32_e32 v215, s62, v71\n"
      "v_pk_fma_f32 v[208:209], v[208:209], s[64:65], v[184:185]\n"
      "v_pk_fma_f32 v[210:211], v[210:211], s[64:65], v[186:187]\n"
      "v_pk_fma_f32 v[212:213], v[212:213], s[64:65], v[188:189]\n"
      "v_pk_fma_f32 v[214:215], v[214:215], s[64:65], v[190:191]\n"
      "v_cvt_pk_bf16_f32 v68, v208, v209\n"
      "v_cvt_pk_bf16_f32 v69, v210, v211\n"
      "v_cvt_pk_bf16_f32 v70, v212, v213\n"
      "v_cvt_pk_bf16_f32 v71, v214, v215\n"
      "v_lshlrev_b32_e32 v208, 16, v72\n"
      "v_lshlrev_b32_e32 v210, 16, v73\n"
      "v_lshlrev_b32_e32 v212, 16, v74\n"
      "v_lshlrev_b32_e32 v214, 16, v75\n"
      "buffer_load_dwordx4 v253, s[48:51], s69 offen offset:2048 lds\n"
      "v_and_b32_e32 v209, s62, v72\n"
      "v_and_b32_e32 v211, s62, v73\n"
      "v_and_b32_e32 v213, s62, v74\n"
      "v_and_b32_e32 v215, s62, v75\n"
      "v_pk_fma_f32 v[208:209], v[208:209], s[64:65], v[192:193]\n"
      "v_pk_fma_f32 v[210:211], v[210:211], s[64:65], v[194:195]\n"
      "v_pk_fma_f32 v[212:213], v[212:213], s[64:65], v[196:197]\n"
      "v_pk_fma_f32 v[214:215], v[214:215], s[64:65], v[198:199]\n"
      "v_cvt_pk_bf16_f32 v72, v208, v209\n"
      "v_cvt_pk_bf16_f32 v73, v210, v211\n"
      "v_cvt_pk_bf16_f32 v74, v212, v213\n"
      "v_cvt_pk_bf16_f32 v75, v214, v215\n"
      "v_lshlrev_b32_e32 v208, 16, v76\n"
      "v_lshlrev_b32_e32 v210, 16, v77\n"
      "v_lshlrev_b32_e32 v212, 16, v78\n"
      "v_lshlrev_b32_e32 v214, 16, v79\n"
      "v_and_b32_e32 v209, s62, v76\n"
      "v_and_b32_e32 v211, s62, v77\n"
      "v_and_b32_e32 v213, s62, v78\n"
      "v_and_b32_e32 v215, s62, v79\n"
      "v_pk_fma_f32 v[208:209], v[208:209], s[64:65], v[200:201]\n"
      "v_pk_fma_f32 v[210:211], v[210:211], s[64:65], v[202:203]\n"
      "v_pk_fma_f32 v[212:213], v[212:213], s[64:65], v[204:205]\n"
      "v_pk_fma_f32 v[214:215], v[214:215], s[64:65], v[206:207]\n"
      "v_cvt_pk_bf16_f32 v76, v208, v209\n"
      "v_cvt_pk_bf16_f32 v77, v210, v211\n"
      "v_cvt_pk_bf16_f32 v78, v212, v213\n"
      "buffer_load_dwordx4 v253, s[48:51], s69 offen offset:3072 lds\n"
      "v_cvt_pk_bf16_f32 v79, v214, v215\n"
      "v_lshlrev_b32_e32 v208, 16, v80\n"
      "v_lshlrev_b32_e32 v210, 16, v81\n"
      "v_lshlrev_b32_e32 v212, 16, v82\n"
      "v_lshlrev_b32_e32 v214, 16, v83\n"
      "v_and_b32_e32 v209, s62, v80\n"
      "v_and_b32_e32 v211, s62, v81\n"
      "v_and_b32_e32 v213, s62, v82\n"
      "v_and_b32_e32 v215, s62, v83\n"
      "v_pk_fma_f32 v[208:209], v[208:209], s[64:65], v[176:177]\n"
      "v_pk_fma_f32 v[210:211], v[210:211], s[64:65], v[178:179]\n"
      "v_pk_fma_f32 v[212:213], v[212:213], s[64:65], v[180:181]\n"
      "v_pk_fma_f32 v[214:215], v[214:215], s[64:65], v[182:183]\n"
      "v_cvt_pk_bf16_f32 v80, v208, v209\n"
      "v_cvt_pk_bf16_f32 v81, v210, v211\n"
      "v_cvt_pk_bf16_f32 v82, v212, v213\n"
      "v_cvt_pk_bf16_f32 v83, v214, v215\n"
      "v_lshlrev_b32_e32 v208, 16, v84\n"
      "v_lshlrev_b32_e32 v210, 16, v85\n"
      "v_lshlrev_b32_e32 v212, 16, v86\n"
      "v_lshlrev_b32_e32 v214, 16, v87\n"
      "v_and_b32_e32 v209, s62, v84\n"
      "v_and_b32_e32 v211, s62, v85\n"
      "v_and_b32_e32 v213, s62, v86\n"
      "v_and_b32_e32 v215, s62, v87\n"
      "v_pk_fma_f32 v[208:209], v[208:209], s[64:65], v[184:185]\n"
      "v_pk_fma_f32 v[210:211], v[210:211], s[64:65], v[186:187]\n"
      "v_pk_fma_f32 v[212:213], v[212:213], s[64:65], v[188:189]\n"
      "s_add_u32 m0, s60, 0x24000\n"
      "s_mov_b32 s58, 0x4000\n"
      "buffer_load_dwordx4 v248, s[40:43], s58 offen offset:0 lds\n"
      "v_pk_fma_f32 v[214:215], v[214:215], s[64:65], v[190:191]\n"
      "v_cvt_pk_bf16_f32 v84, v208, v209\n"
      "v_cvt_pk_bf16_f32 v85, v210, v211\n"
      "v_cvt_pk_bf16_f32 v86, v212, v213\n"
      "v_cvt_pk_bf16_f32 v87, v214, v215\n"
      "v_lshlrev_b32_e32 v208, 16, v88\n"
      "v_lshlrev_b32_e32 v210, 16, v89\n"
      "v_lshlrev_b32_e32 v212, 16, v90\n"
      "v_lshlrev_b32_e32 v214, 16, v91\n"
      "v_and_b32_e32 v209, s62, v88\n"
      "v_and_b32_e32 v211, s62, v89\n"
      "v_and_b32_e32 v213, s62, v90\n"
      "v_and_b32_e32 v215, s62, v91\n"
      "v_pk_fma_f32 v[208:209], v[208:209], s[64:65], v[192:193]\n"
      "v_pk_fma_f32 v[210:211], v[210:211], s[64:65], v[194:195]\n"
      "v_pk_fma_f32 v[212:213], v[212:213], s[64:65], v[196:197]\n"
      "v_pk_fma_f32 v[214:215], v[214:215], s[64:65], v[198:199]\n"
      "v_cvt_pk_bf16_f32 v88, v208, v209\n"
      "v_cvt_pk_bf16_f32 v89, v210, v211\n"
      "v_cvt_pk_bf16_f32 v90, v212, v213\n"
      "v_cvt_pk_bf16_f32 v91, v214, v215\n"
      "v_lshlrev_b32_e32 v208, 16, v92\n"
      "v_lshlrev_b32_e32 v210, 16, v93\n"
      "v_lshlrev_b32_e32 v212, 16, v94\n"
      "v_lshlrev_b32_e32 v214, 16, v95\n"
      "v_and_b32_e32 v209, s62, v92\n"
      "v_and_b32_e32 v211, s62, v93\n"
      "buffer_load_dwordx4 v248, s[40:43], s58 offen offset:1024 lds\n"
      "v_and_b32_e32 v213, s62, v94\n"
      "v_and_b32_e32 v215, s62, v95\n"
      "v_pk_fma_f32 v[208:209], v[208:209], s[64:65], v[200:201]\n"
      "v_pk_fma_f32 v[210:211], v[210:211], s[64:65], v[202:203]\n"
      "v_pk_fma_f32 v[212:213], v[212:213], s[64:65], v[204:205]\n"
      "v_pk_fma_f32 v[214:215], v[214:215], s[64:65], v[206:207]\n"
      "v_cvt_pk_bf16_f32 v92, v208, v209\n"
      "v_cvt_pk_bf16_f32 v93, v210, v211\n"
      "v_cvt_pk_bf16_f32 v94, v212, v213\n"
      "v_cvt_pk_bf16_f32 v95, v214, v215\n"
      "v_mfma_f32_16x16x32_bf16 v[128:131], v[32:35], a[192:195], 0\n"
      "v_cvt_pk_bf16_f32 v168, v136, v137\n"
      "v_mfma_f32_16x16x32_bf16 v[128:131], v[36:39], a[196:199], v[128:131]\n"
      "v_exp_f32_e32 v141, v141\n"
      "v_mfma_f32_16x16x32_bf16 v[128:131], v[40:43], a[200:203], v[128:131]\n"
      "v_cvt_pk_bf16_f32 v169, v138, v139\n"
      "v_mfma_f32_16x16x32_bf16 v[128:131], v[44:47], a[204:207], v[128:131]\n"
      "v_exp_f32_e32 v142, v142\n"
      "v_mfma_f32_16x16x32_bf16 v[132:135], v[60:63], a[204:207], 0\n"
      "s_add_u32 m0, s57, 0x4000\n"
      "s_mov_b32 s69, 0x2000\n"
      "buffer_load_dwordx4 v253, s[48:51], s69 offen offset:0 lds\n"
      "v_cvt_pk_bf16_f32 v170, v140, v141\n"
      "v_mfma_f32_16x16x32_bf16 v[132:135], v[56:59], a[200:203], v[132:135]\n"
      "v_exp_f32_e32 v143, v143\n"
      "v_mfma_f32_16x16x32_bf16 v[132:135], v[52:55], a[196:199], v[132:135]\n"
      "v_cvt_pk_bf16_f32 v171, v142, v143\n"
      "v_mfma_f32_16x16x32_bf16 v[132:135], v[48:51], a[192:195], v[132:135]\n"
      "v_exp_f32_e32 v128, v128\n"
      "s_waitcnt vmcnt(11)\n"
      "ds_read_b128 v[96:99], v251 offset:24576\n"
      "ds_read_b128 v[100:103], v251 offset:24832\n"
      "ds_read_b128 v[104:107], v251 offset:25088\n"
      "ds_read_b128 v[108:111], v251 offset:25344\n"
      "ds_read_b128 v[112:115], v251 offset:28672\n"
      "ds_read_b128 v[116:119], v251 offset:28928\n"
      "ds_read_b128 v[120:123], v251 offset:29184\n"
      "buffer_load_dwordx4 v253, s[48:51], s69 offen offset:1024 lds\n"
      "ds_read_b128 v[124:127], v251 offset:29440\n"
      "ds_write_b128 v242, v[168:171] offset:5120\n"
      "v_exp_f32_e32 v129, v129\n"
      "v_exp_f32_e32 v130, v130\n"
      "v_exp_f32_e32 v131, v131\n"
      "s_waitcnt lgkmcnt(1)\n"
      "v_exp_f32_e32 v132, v132\n"
      "v_mfma_f32_16x16x32_bf16 v[136:139], v[32:35], a[208:211], 0\n"
      "v_cvt_pk_bf16_f32 v156, v128, v129\n"
      "v_mfma_f32_16x16x32_bf16 v[136:139], v[36:39], a[212:215], v[136:139]\n"
      "v_exp_f32_e32 v133, v133\n"
      "v_mfma_f32_16x16x32_bf16 v[136:139], v[40:43], a[216:219], v[136:139]\n"
      "buffer_load_dwordx4 v253, s[48:51], s69 offen offset:2048 lds\n"
      "v_cvt_pk_bf16_f32 v157, v130, v131\n"
      "v_mfma_f32_16x16x32_bf16 v[136:139], v[44:47], a[220:223], v[136:139]\n"
      "v_exp_f32_e32 v134, v134\n"
      "v_mfma_f32_16x16x32_bf16 v[140:143], v[60:63], a[220:223], 0\n"
      "v_cvt_pk_bf16_f32 v158, v132, v133\n"
      "v_mfma_f32_16x16x32_bf16 v[140:143], v[56:59], a[216:219], v[140:143]\n"
      "v_exp_f32_e32 v135, v135\n"
      "v_mfma_f32_16x16x32_bf16 v[140:143], v[52:55], a[212:215], v[140:143]\n"
      "v_cvt_pk_bf16_f32 v159, v134, v135\n"
      "v_mfma_f32_16x16x32_bf16 v[140:143], v[48:51], a[208:211], v[140:143]\n"
      "v_exp_f32_e32 v136, v136\n"
      "v_smfmac_f32_16x16x64_bf16 v[228:231], v[216:219], v[152:159], v241\n"
      "ds_write_b128 v242, v[156:159] offset:6144\n"
      "buffer_load_dwordx4 v253, s[48:51], s69 offen offset:3072 lds\n"
      "v_exp_f32_e32 v137, v137\n"
      "v_exp_f32_e32 v138, v138\n"
      "v_exp_f32_e32 v139, v139\n"
      "s_nop 0\n"
      "v_exp_f32_e32 v140, v140\n"
      "v_mfma_f32_16x16x32_bf16 v[128:131], v[64:67], a[128:131], 0\n"
      "v_cvt_pk_bf16_f32 v172, v136, v137\n"
      "v_mfma_f32_16x16x32_bf16 v[128:131], v[68:71], a[132:135], v[128:131]\n"
      "v_exp_f32_e32 v141, v141\n"
      "v_mfma_f32_16x16x32_bf16 v[128:131], v[72:75], a[136:139], v[128:131]\n"
      "v_cvt_pk_bf16_f32 v173, v138, v139\n"
      "v_mfma_f32_16x16x32_bf16 v[128:131], v[76:79], a[140:143], v[128:131]\n"
      "v_exp_f32_e32 v142, v142\n"
      "v_mfma_f32_16x16x32_bf16 v[132:135], v[92:95], a[140:143], 0\n"
      "s_add_u32 m0, s57, 0x5000\n"
      "s_mov_b32 s69, 0x3000\n"
      "buffer_load_dwordx4 v253, s[48:51], s69 offen offset:0 lds\n"
      "v_cvt_pk_bf16_f32 v174, v140, v141\n"
      "v_mfma_f32_16x16x32_bf16 v[132:135], v[88:91], a[136:139], v[132:135]\n"
      "v_exp_f32_e32 v143, v143\n"
      "v_mfma_f32_16x16x32_bf16 v[132:135], v[84:87], a[132:135], v[132:135]\n"
      "v_cvt_pk_bf16_f32 v175, v142, v143\n"
      "v_mfma_f32_16x16x32_bf16 v[132:135], v[80:83], a[128:131], v[132:135]\n"
      "v_exp_f32_e32 v128, v128\n"
      "v_smfmac_f32_16x16x64_bf16 v[228:231], v[220:223], v[168:175], v241\n"
      "ds_write_b128 v242, v[172:175] offset:7168\n"
      "v_xor_b32_e32 v240, 0x4000, v240\n"
      "v_exp_f32_e32 v129, v129\n"
      "v_exp_f32_e32 v130, v130\n"
      "v_exp_f32_e32 v131, v131\n"
      "s_nop 0\n"
      "v_exp_f32_e32 v132, v132\n"
      "buffer_load_dwordx4 v253, s[48:51], s69 offen offset:1024 lds\n"
      "v_mfma_f32_16x16x32_bf16 v[136:139], v[64:67], a[144:147], 0\n"
      "v_cvt_pk_bf16_f32 v144, v128, v129\n"
      "v_mfma_f32_16x16x32_bf16 v[136:139], v[68:71], a[148:151], v[136:139]\n"
      "v_exp_f32_e32 v133, v133\n"
      "v_mfma_f32_16x16x32_bf16 v[136:139], v[72:75], a[152:155], v[136:139]\n"
      "v_cvt_pk_bf16_f32 v145, v130, v131\n"
      "v_mfma_f32_16x16x32_bf16 v[136:139], v[76:79], a[156:159], v[136:139]\n"
      "v_exp_f32_e32 v134, v134\n"
      "v_mfma_f32_16x16x32_bf16 v[140:143], v[92:95], a[156:159], 0\n"
      "v_cvt_pk_bf16_f32 v146, v132, v133\n"
      "v_mfma_f32_16x16x32_bf16 v[140:143], v[88:91], a[152:155], v[140:143]\n"
      "v_exp_f32_e32 v135, v135\n"
      "v_mfma_f32_16x16x32_bf16 v[140:143], v[84:87], a[148:151], v[140:143]\n"
      "buffer_load_dwordx4 v253, s[48:51], s69 offen offset:2048 lds\n"
      "v_cvt_pk_bf16_f32 v147, v134, v135\n"
      "v_mfma_f32_16x16x32_bf16 v[140:143], v[80:83], a[144:147], v[140:143]\n"
      "v_exp_f32_e32 v136, v136\n"
      "v_exp_f32_e32 v137, v137\n"
      "v_exp_f32_e32 v138, v138\n"
      "v_exp_f32_e32 v139, v139\n"
      "s_nop 3\n"
      "v_exp_f32_e32 v140, v140\n"
      "v_lshlrev_b32_e32 v208, 16, v96\n"
      "v_lshlrev_b32_e32 v210, 16, v97\n"
      "v_lshlrev_b32_e32 v212, 16, v98\n"
      "v_lshlrev_b32_e32 v214, 16, v99\n"
      "v_and_b32_e32 v209, s62, v96\n"
      "v_and_b32_e32 v211, s62, v97\n"
      "v_and_b32_e32 v213, s62, v98\n"
      "v_and_b32_e32 v215, s62, v99\n"
      "v_pk_fma_f32 v[208:209], v[208:209], s[64:65], v[176:177]\n"
      "v_pk_fma_f32 v[210:211], v[210:211], s[64:65], v[178:179]\n"
      "v_pk_fma_f32 v[212:213], v[212:213], s[64:65], v[180:181]\n"
      "v_pk_fma_f32 v[214:215], v[214:215], s[64:65], v[182:183]\n"
      "v_cvt_pk_bf16_f32 v96, v208, v209\n"
      "v_cvt_pk_bf16_f32 v97, v210, v211\n"
      "v_cvt_pk_bf16_f32 v98, v212, v213\n"
      "v_cvt_pk_bf16_f32 v99, v214, v215\n"
      "v_lshlrev_b32_e32 v208, 16, v100\n"
      "buffer_load_dwordx4 v253, s[48:51], s69 offen offset:3072 lds\n"
      "v_lshlrev_b32_e32 v210, 16, v101\n"
      "v_lshlrev_b32_e32 v212, 16, v102\n"
      "v_lshlrev_b32_e32 v214, 16, v103\n"
      "v_and_b32_e32 v209, s62, v100\n"
      "v_and_b32_e32 v211, s62, v101\n"
      "v_and_b32_e32 v213, s62, v102\n"
      "v_and_b32_e32 v215, s62, v103\n"
      "v_pk_fma_f32 v[208:209], v[208:209], s[64:65], v[184:185]\n"
      "v_pk_fma_f32 v[210:211], v[210:211], s[64:65], v[186:187]\n"
      "v_pk_fma_f32 v[212:213], v[212:213], s[64:65], v[188:189]\n"
      "v_pk_fma_f32 v[214:215], v[214:215], s[64:65], v[190:191]\n"
      "v_cvt_pk_bf16_f32 v100, v208, v209\n"
      "v_cvt_pk_bf16_f32 v101, v210, v211\n"
      "v_cvt_pk_bf16_f32 v102, v212, v213\n"
      "v_cvt_pk_bf16_f32 v103, v214, v215\n"
      "v_lshlrev_b32_e32 v208, 16, v104\n"
      "v_lshlrev_b32_e32 v210, 16, v105\n"
      "v_lshlrev_b32_e32 v212, 16, v106\n"
      "v_lshlrev_b32_e32 v214, 16, v107\n"
      "v_and_b32_e32 v209, s62, v104\n"
      "v_and_b32_e32 v211, s62, v105\n"
      "v_and_b32_e32 v213, s62, v106\n"
      "v_and_b32_e32 v215, s62, v107\n"
      "v_pk_fma_f32 v[208:209], v[208:209], s[64:65], v[192:193]\n"
      "v_pk_fma_f32 v[210:211], v[210:211], s[64:65], v[194:195]\n"
      "v_pk_fma_f32 v[212:213], v[212:213], s[64:65], v[196:197]\n"
      "v_pk_fma_f32 v[214:215], v[214:215], s[64:65], v[198:199]\n"
      "s_add_u32 m0, s57, 0x6000\n"
      "s_mov_b32 s69, 0x4000\n"
      "buffer_load_dwordx4 v253, s[48:51], s69 offen offset:0 lds\n"
      "v_cvt_pk_bf16_f32 v104, v208, v209\n"
      "v_cvt_pk_bf16_f32 v105, v210, v211\n"
      "v_cvt_pk_bf16_f32 v106, v212, v213\n"
      "v_cvt_pk_bf16_f32 v107, v214, v215\n"
      "v_lshlrev_b32_e32 v208, 16, v108\n"
      "v_lshlrev_b32_e32 v210, 16, v109\n"
      "v_lshlrev_b32_e32 v212, 16, v110\n"
      "v_lshlrev_b32_e32 v214, 16, v111\n"
      "v_and_b32_e32 v209, s62, v108\n"
      "v_and_b32_e32 v211, s62, v109\n"
      "v_and_b32_e32 v213, s62, v110\n"
      "v_and_b32_e32 v215, s62, v111\n"
      "v_pk_fma_f32 v[208:209], v[208:209], s[64:65], v[200:201]\n"
      "v_pk_fma_f32 v[210:211], v[210:211], s[64:65], v[202:203]\n"
      "v_pk_fma_f32 v[212:213], v[212:213], s[64:65], v[204:205]\n"
      "v_pk_fma_f32 v[214:215], v[214:215], s[64:65], v[206:207]\n"
      "v_cvt_pk_bf16_f32 v108, v208, v209\n"
      "v_cvt_pk_bf16_f32 v109, v210, v211\n"
      "v_cvt_pk_bf16_f32 v110, v212, v213\n"
      "v_cvt_pk_bf16_f32 v111, v214, v215\n"
      "v_lshlrev_b32_e32 v208, 16, v112\n"
      "v_lshlrev_b32_e32 v210, 16, v113\n"
      "v_lshlrev_b32_e32 v212, 16, v114\n"
      "v_lshlrev_b32_e32 v214, 16, v115\n"
      "v_and_b32_e32 v209, s62, v112\n"
      "v_and_b32_e32 v211, s62, v113\n"
      "v_and_b32_e32 v213, s62, v114\n"
      "v_and_b32_e32 v215, s62, v115\n"
      "buffer_load_dwordx4 v253, s[48:51], s69 offen offset:1024 lds\n"
      "v_pk_fma_f32 v[208:209], v[208:209], s[64:65], v[176:177]\n"
      "v_pk_fma_f32 v[210:211], v[210:211], s[64:65], v[178:179]\n"
      "v_pk_fma_f32 v[212:213], v[212:213], s[64:65], v[180:181]\n"
      "v_pk_fma_f32 v[214:215], v[214:215], s[64:65], v[182:183]\n"
      "v_cvt_pk_bf16_f32 v112, v208, v209\n"
      "v_cvt_pk_bf16_f32 v113, v210, v211\n"
      "v_cvt_pk_bf16_f32 v114, v212, v213\n"
      "v_cvt_pk_bf16_f32 v115, v214, v215\n"
      "v_lshlrev_b32_e32 v208, 16, v116\n"
      "v_lshlrev_b32_e32 v210, 16, v117\n"
      "v_lshlrev_b32_e32 v212, 16, v118\n"
      "v_lshlrev_b32_e32 v214, 16, v119\n"
      "v_and_b32_e32 v209, s62, v116\n"
      "v_and_b32_e32 v211, s62, v117\n"
      "v_and_b32_e32 v213, s62, v118\n"
      "v_and_b32_e32 v215, s62, v119\n"
      "v_pk_fma_f32 v[208:209], v[208:209], s[64:65], v[184:185]\n"
      "v_pk_fma_f32 v[210:211], v[210:211], s[64:65], v[186:187]\n"
      "v_pk_fma_f32 v[212:213], v[212:213], s[64:65], v[188:189]\n"
      "v_pk_fma_f32 v[214:215], v[214:215], s[64:65], v[190:191]\n"
      "v_cvt_pk_bf16_f32 v116, v208, v209\n"
      "v_cvt_pk_bf16_f32 v117, v210, v211\n"
      "v_cvt_pk_bf16_f32 v118, v212, v213\n"
      "v_cvt_pk_bf16_f32 v119, v214, v215\n"
      "v_lshlrev_b32_e32 v208, 16, v120\n"
      "v_lshlrev_b32_e32 v210, 16, v121\n"
      "v_lshlrev_b32_e32 v212, 16, v122\n"
      "buffer_load_dwordx4 v253, s[48:51], s69 offen offset:2048 lds\n"
      "v_lshlrev_b32_e32 v214, 16, v123\n"
      "v_and_b32_e32 v209, s62, v120\n"
      "v_and_b32_e32 v211, s62, v121\n"
      "v_and_b32_e32 v213, s62, v122\n"
      "v_and_b32_e32 v215, s62, v123\n"
      "v_pk_fma_f32 v[208:209], v[208:209], s[64:65], v[192:193]\n"
      "v_pk_fma_f32 v[210:211], v[210:211], s[64:65], v[194:195]\n"
      "v_pk_fma_f32 v[212:213], v[212:213], s[64:65], v[196:197]\n"
      "v_pk_fma_f32 v[214:215], v[214:215], s[64:65], v[198:199]\n"
      "v_cvt_pk_bf16_f32 v120, v208, v209\n"
      "v_cvt_pk_bf16_f32 v121, v210, v211\n"
      "v_cvt_pk_bf16_f32 v122, v212, v213\n"
      "v_cvt_pk_bf16_f32 v123, v214, v215\n"
      "v_lshlrev_b32_e32 v208, 16, v124\n"
      "v_lshlrev_b32_e32 v210, 16, v125\n"
      "v_lshlrev_b32_e32 v212, 16, v126\n"
      "v_lshlrev_b32_e32 v214, 16, v127\n"
      "v_and_b32_e32 v209, s62, v124\n"
      "v_and_b32_e32 v211, s62, v125\n"
      "v_and_b32_e32 v213, s62, v126\n"
      "v_and_b32_e32 v215, s62, v127\n"
      "v_pk_fma_f32 v[208:209], v[208:209], s[64:65], v[200:201]\n"
      "v_pk_fma_f32 v[210:211], v[210:211], s[64:65], v[202:203]\n"
      "v_pk_fma_f32 v[212:213], v[212:213], s[64:65], v[204:205]\n"
      "v_pk_fma_f32 v[214:215], v[214:215], s[64:65], v[206:207]\n"
      "v_cvt_pk_bf16_f32 v124, v208, v209\n"
      "v_cvt_pk_bf16_f32 v125, v210, v211\n"
      "v_cvt_pk_bf16_f32 v126, v212, v213\n"
      "buffer_load_dwordx4 v253, s[48:51], s69 offen offset:3072 lds\n"
      "v_cvt_pk_bf16_f32 v127, v214, v215\n"
      "v_mfma_f32_16x16x32_bf16 v[128:131], v[96:99], a[128:131], 0\n"
      "v_cvt_pk_bf16_f32 v160, v136, v137\n"
      "v_mfma_f32_16x16x32_bf16 v[128:131], v[100:103], a[132:135], v[128:131]\n"
      "v_exp_f32_e32 v141, v141\n"
      "v_mfma_f32_16x16x32_bf16 v[128:131], v[104:107], a[136:139], v[128:131]\n"
      "v_cvt_pk_bf16_f32 v161, v138, v139\n"
      "v_mfma_f32_16x16x32_bf16 v[128:131], v[108:111], a[140:143], v[128:131]\n"
      "v_exp_f32_e32 v142, v142\n"
      "v_mfma_f32_16x16x32_bf16 v[132:135], v[124:127], a[140:143], 0\n"
      "v_cvt_pk_bf16_f32 v162, v140, v141\n"
      "v_mfma_f32_16x16x32_bf16 v[132:135], v[120:123], a[136:139], v[132:135]\n"
      "v_exp_f32_e32 v143, v143\n"
      "v_mfma_f32_16x16x32_bf16 v[132:135], v[116:119], a[132:135], v[132:135]\n"
      "s_add_u32 m0, s57, 0x7000\n"
      "s_mov_b32 s69, 0x5000\n"
      "buffer_load_dwordx4 v253, s[48:51], s69 offen offset:0 lds\n"
      "v_cvt_pk_bf16_f32 v163, v142, v143\n"
      "v_mfma_f32_16x16x32_bf16 v[132:135], v[112:115], a[128:131], v[132:135]\n"
      "v_exp_f32_e32 v128, v128\n"
      "v_exp_f32_e32 v129, v129\n"
      "v_exp_f32_e32 v130, v130\n"
      "v_exp_f32_e32 v131, v131\n"
      "s_nop 3\n"
      "v_exp_f32_e32 v132, v132\n"
      "v_mfma_f32_16x16x32_bf16 v[136:139], v[96:99], a[144:147], 0\n"
      "v_cvt_pk_bf16_f32 v148, v128, v129\n"
      "v_mfma_f32_16x16x32_bf16 v[136:139], v[100:103], a[148:151], v[136:139]\n"
      "v_exp_f32_e32 v133, v133\n"
      "v_mfma_f32_16x16x32_bf16 v[136:139], v[104:107], a[152:155], v[136:139]\n"
      "v_cvt_pk_bf16_f32 v149, v130, v131\n"
      "v_mfma_f32_16x16x32_bf16 v[136:139], v[108:111], a[156:159], v[136:139]\n"
      "buffer_load_dwordx4 v253, s[48:51], s69 offen offset:1024 lds\n"
      "v_exp_f32_e32 v134, v134\n"
      "v_mfma_f32_16x16x32_bf16 v[140:143], v[124:127], a[156:159], 0\n"
      "v_cvt_pk_bf16_f32 v150, v132, v133\n"
      "v_mfma_f32_16x16x32_bf16 v[140:143], v[120:123], a[152:155], v[140:143]\n"
      "v_exp_f32_e32 v135, v135\n"
      "v_mfma_f32_16x16x32_bf16 v[140:143], v[116:119], a[148:151], v[140:143]\n"
      "v_cvt_pk_bf16_f32 v151, v134, v135\n"
      "v_mfma_f32_16x16x32_bf16 v[140:143], v[112:115], a[144:147], v[140:143]\n"
      "v_exp_f32_e32 v136, v136\n"
      "v_smfmac_f32_16x16x64_bf16 v[224:227], v[216:219], v[144:151], v241\n"
      "v_exp_f32_e32 v137, v137\n"
      "v_exp_f32_e32 v138, v138\n"
      "v_exp_f32_e32 v139, v139\n"
      "s_nop 2\n"
      "v_exp_f32_e32 v140, v140\n"
      "s_waitcnt vmcnt(14) lgkmcnt(0)\n"
      "s_barrier\n"
      "buffer_load_dwordx4 v253, s[48:51], s69 offen offset:2048 lds\n"
      "v_mfma_f32_16x16x32_bf16 v[128:131], v[64:67], a[192:195], 0\n"
      "ds_read_b64_tr_b16 a[0:1], v236 offset:8192\n"
      "v_cvt_pk_bf16_f32 v164, v136, v137\n"
      "v_mfma_f32_16x16x32_bf16 v[128:131], v[68:71], a[196:199], v[128:131]\n"
      "v_exp_f32_e32 v141, v141\n"
      "v_mfma_f32_16x16x32_bf16 v[128:131], v[72:75], a[200:203], v[128:131]\n"
      "ds_read_b64_tr_b16 a[2:3], v236 offset:12288\n"
      "v_cvt_pk_bf16_f32 v165, v138, v139\n"
      "v_mfma_f32_16x16x32_bf16 v[128:131], v[76:79], a[204:207], v[128:131]\n"
      "buffer_load_dwordx4 v253, s[48:51], s69 offen offset:3072 lds\n"
      "v_exp_f32_e32 v142, v142\n"
      "v_mfma_f32_16x16x32_bf16 v[132:135], v[92:95], a[204:207], 0\n"
      "ds_read_b64_tr_b16 a[4:5], v236 offset:8200\n"
      "v_cvt_pk_bf16_f32 v166, v140, v141\n"
      "v_mfma_f32_16x16x32_bf16 v[132:135], v[88:91], a[200:203], v[132:135]\n"
      "v_exp_f32_e32 v143, v143\n"
      "v_mfma_f32_16x16x32_bf16 v[132:135], v[84:87], a[196:199], v[132:135]\n"
      "ds_read_b64_tr_b16 a[6:7], v236 offset:12296\n"
      "v_cvt_pk_bf16_f32 v167, v142, v143\n"
      "v_mfma_f32_16x16x32_bf16 v[132:135], v[80:83], a[192:195], v[132:135]\n"
      "v_exp_f32_e32 v128, v128\n"
      "v_smfmac_f32_16x16x64_bf16 v[224:227], v[220:223], v[160:167], v241\n"
      "ds_read_b64_tr_b16 a[32:33], v237 offset:8192\n"
      "ds_read_b64_tr_b16 a[34:35], v237 offset:12288\n"
      "v_exp_f32_e32 v129, v129\n"
      "ds_read_b64_tr_b16 a[36:37], v237 offset:8200\n"
      "s_add_u32 m0, s60, 0x26000\n"
      "s_mov_b32 s58, 0x6000\n"
      "buffer_load_dwordx4 v248, s[40:43], s58 offen offset:0 lds\n"
      "ds_read_b64_tr_b16 a[38:39], v237 offset:12296\n"
      "v_exp_f32_e32 v130, v130\n"
      "ds_read_b64_tr_b16 a[64:65], v238 offset:8192\n"
      "ds_read_b64_tr_b16 a[66:67], v238 offset:12288\n"
      "v_exp_f32_e32 v131, v131\n"
      "ds_read_b64_tr_b16 a[68:69], v238 offset:8200\n"
      "v_exp_f32_e32 v132, v132\n"
      "v_mfma_f32_16x16x32_bf16 v[136:139], v[64:67], a[208:211], 0\n"
      "ds_read_b64_tr_b16 a[70:71], v238 offset:12296\n"
      "v_cvt_pk_bf16_f32 v152, v128, v129\n"
      "v_mfma_f32_16x16x32_bf16 v[136:139], v[68:71], a[212:215], v[136:139]\n"
      "v_exp_f32_e32 v133, v133\n"
      "v_mfma_f32_16x16x32_bf16 v[136:139], v[72:75], a[216:219], v[136:139]\n"
      "buffer_load_dwordx4 v248, s[40:43], s58 offen offset:1024 lds\n"
      "ds_read_b64_tr_b16 a[96:97], v239 offset:8192\n"
      "v_cvt_pk_bf16_f32 v153, v130, v131\n"
      "v_mfma_f32_16x16x32_bf16 v[136:139], v[76:79], a[220:223], v[136:139]\n"
      "v_exp_f32_e32 v134, v134\n"
      "v_mfma_f32_16x16x32_bf16 v[140:143], v[92:95], a[220:223], 0\n"
      "ds_read_b64_tr_b16 a[98:99], v239 offset:12288\n"
      "v_cvt_pk_bf16_f32 v154, v132, v133\n"
      "v_mfma_f32_16x16x32_bf16 v[140:143], v[88:91], a[216:219], v[140:143]\n"
      "v_exp_f32_e32 v135, v135\n"
      "v_mfma_f32_16x16x32_bf16 v[140:143], v[84:87], a[212:215], v[140:143]\n"
      "ds_read_b64_tr_b16 a[100:101], v239 offset:8200\n"
      "v_cvt_pk_bf16_f32 v155, v134, v135\n"
      "v_mfma_f32_16x16x32_bf16 v[140:143], v[80:83], a[208:211], v[140:143]\n"
      "v_exp_f32_e32 v136, v136\n"
      "ds_read_b64_tr_b16 a[102:103], v239 offset:12296\n"
      "v_exp_f32_e32 v137, v137\n"
      "v_exp_f32_e32 v138, v138\n"
      "v_exp_f32_e32 v139, v139\n"
      "s_nop 2\n"
      "v_exp_f32_e32 v140, v140\n"
      "s_waitcnt lgkmcnt(0)\n"
      "s_barrier\n"
      "v_mfma_f32_16x16x32_bf16 v[128:131], v[96:99], a[192:195], 0\n"
      "s_add_u32 m0, s57, 0x2000\n"
      "s_mov_b32 s69, 0x6000\n"
      "buffer_load_dwordx4 v253, s[48:51], s69 offen offset:0 lds\n"
      "ds_read_b128 a[128:131], v240 offset:0\n"
      "v_cvt_pk_bf16_f32 v168, v136, v137\n"
      "v_mfma_f32_16x16x32_bf16 v[128:131], v[100:103], a[196:199], v[128:131]\n"
      "v_exp_f32_e32 v141, v141\n"
      "v_mfma_f32_16x16x32_bf16 v[128:131], v[104:107], a[200:203], v[128:131]\n"
      "buffer_load_dwordx4 v253, s[48:51], s69 offen offset:1024 lds\n"
      "ds_read_b128 a[132:135], v240 offset:256\n"
      "v_cvt_pk_bf16_f32 v169, v138, v139\n"
      "v_mfma_f32_16x16x32_bf16 v[128:131], v[108:111], a[204:207], v[128:131]\n"
      "v_exp_f32_e32 v142, v142\n"
      "v_mfma_f32_16x16x32_bf16 v[132:135], v[124:127], a[204:207], 0\n"
      "ds_read_b128 a[136:139], v240 offset:512\n"
      "v_cvt_pk_bf16_f32 v170, v140, v141\n"
      "v_mfma_f32_16x16x32_bf16 v[132:135], v[120:123], a[200:203], v[132:135]\n"
      "v_exp_f32_e32 v143, v143\n"
      "v_mfma_f32_16x16x32_bf16 v[132:135], v[116:119], a[196:199], v[132:135]\n"
      "ds_read_b128 a[140:143], v240 offset:768\n"
      "v_cvt_pk_bf16_f32 v171, v142, v143\n"
      "v_mfma_f32_16x16x32_bf16 v[132:135], v[112:115], a[192:195], v[132:135]\n"
      "v_exp_f32_e32 v128, v128\n"
      "ds_read_b128 a[144:147], v240 offset:4096\n"
      "v_mfma_f32_16x16x32_bf16 a[240:243], a[0:3], a[0:3], 0\n"
      "buffer_load_dwordx4 v253, s[48:51], s69 offen offset:2048 lds\n"
      "v_exp_f32_e32 v129, v129\n"
      "ds_read_b128 a[148:151], v240 offset:4352\n"
      "v_mfma_f32_16x16x32_bf16 a[240:243], a[4:7], a[4:7], a[240:243]\n"
      "v_exp_f32_e32 v130, v130\n"
      "ds_read_b128 a[152:155], v240 offset:4608\n"
      "v_mfma_f32_16x16x32_bf16 a[240:243], a[32:35], a[32:35], a[240:243]\n"
      "v_exp_f32_e32 v131, v131\n"
      "ds_read_b128 a[156:159], v240 offset:4864\n"
      "v_mfma_f32_16x16x32_bf16 a[240:243], a[36:39], a[36:39], a[240:243]\n"
      "v_exp_f32_e32 v132, v132\n"
      "v_mfma_f32_16x16x32_bf16 v[136:139], v[96:99], a[208:211], 0\n"
      "ds_read_b128 a[160:163], v242 offset:0\n"
      "v_cvt_pk_bf16_f32 v156, v128, v129\n"
      "v_mfma_f32_16x16x32_bf16 v[136:139], v[100:103], a[212:215], v[136:139]\n"
      "buffer_load_dwordx4 v253, s[48:51], s69 offen offset:3072 lds\n"
      "v_exp_f32_e32 v133, v133\n"
      "v_mfma_f32_16x16x32_bf16 v[136:139], v[104:107], a[216:219], v[136:139]\n"
      "ds_read_b128 a[164:167], v242 offset:2048\n"
      "v_cvt_pk_bf16_f32 v157, v130, v131\n"
      "v_mfma_f32_16x16x32_bf16 v[136:139], v[108:111], a[220:223], v[136:139]\n"
      "v_exp_f32_e32 v134, v134\n"
      "v_mfma_f32_16x16x32_bf16 v[140:143], v[124:127], a[220:223], 0\n"
      "ds_read_b128 a[168:171], v242 offset:4096\n"
      "v_cvt_pk_bf16_f32 v158, v132, v133\n"
      "v_mfma_f32_16x16x32_bf16 v[140:143], v[120:123], a[216:219], v[140:143]\n"
      "v_exp_f32_e32 v135, v135\n"
      "v_mfma_f32_16x16x32_bf16 v[140:143], v[116:119], a[212:215], v[140:143]\n"
      "ds_read_b128 a[172:175], v242 offset:6144\n"
      "v_cvt_pk_bf16_f32 v159, v134, v135\n"
      "v_mfma_f32_16x16x32_bf16 v[140:143], v[112:115], a[208:211], v[140:143]\n"
      "v_exp_f32_e32 v136, v136\n"
      "v_smfmac_f32_16x16x64_bf16 v[228:231], v[216:219], v[152:159], v241\n"
      "v_mfma_f32_16x16x32_bf16 a[240:243], a[64:67], a[64:67], a[240:243]\n"
      "v_exp_f32_e32 v137, v137\n"
      "v_mfma_f32_16x16x32_bf16 a[240:243], a[68:71], a[68:71], a[240:243]\n"
      "v_exp_f32_e32 v138, v138\n"
      "v_mfma_f32_16x16x32_bf16 a[240:243], a[96:99], a[96:99], a[240:243]\n"
      "v_exp_f32_e32 v139, v139\n"
      "v_mfma_f32_16x16x32_bf16 a[240:243], a[100:103], a[100:103], a[240:243]\n"
      "v_exp_f32_e32 v140, v140\n"
      "s_waitcnt vmcnt(14)\n"
      "s_barrier\n"
      "ds_read_b64_tr_b16 a[8:9], v236 offset:16384\n"
      "ds_read_b64_tr_b16 a[10:11], v236 offset:20480\n"
      "ds_read_b64_tr_b16 a[12:13], v236 offset:16392\n"
      "ds_read_b64_tr_b16 a[14:15], v236 offset:20488\n"
      "ds_read_b64_tr_b16 a[40:41], v237 offset:16384\n"
      "ds_read_b64_tr_b16 a[42:43], v237 offset:20480\n"
      "ds_read_b64_tr_b16 a[44:45], v237 offset:16392\n"
      "ds_read_b64_tr_b16 a[46:47], v237 offset:20488\n"
      "ds_read_b64_tr_b16 a[72:73], v238 offset:16384\n"
      "ds_read_b64_tr_b16 a[74:75], v238 offset:20480\n"
      "ds_read_b64_tr_b16 a[76:77], v238 offset:16392\n"
      "ds_read_b64_tr_b16 a[78:79], v238 offset:20488\n"
      "ds_read_b64_tr_b16 a[104:105], v239 offset:16384\n"
      "ds_read_b64_tr_b16 a[106:107], v239 offset:20480\n"
      "ds_read_b64_tr_b16 a[108:109], v239 offset:16392\n"
      "ds_read_b64_tr_b16 a[110:111], v239 offset:20488\n"
      "s_waitcnt lgkmcnt(15)\n"
      "v_mfma_f32_16x16x32_bf16 v[128:131], v[0:3], a[128:131], 0\n"
      "ds_read_b128 a[176:179], v242 offset:1024\n"
      "v_cvt_pk_bf16_f32 v172, v136, v137\n"
      "v_mfma_f32_16x16x32_bf16 v[128:131], v[4:7], a[132:135], v[128:131]\n"
      "v_exp_f32_e32 v141, v141\n"
      "v_mfma_f32_16x16x32_bf16 v[128:131], v[8:11], a[136:139], v[128:131]\n"
      "ds_read_b128 a[180:183], v242 offset:3072\n"
      "v_cvt_pk_bf16_f32 v173, v138, v139\n"
      "v_mfma_f32_16x16x32_bf16 v[128:131], v[12:15], a[140:143], v[128:131]\n"
      "v_exp_f32_e32 v142, v142\n"
      "v_mfma_f32_16x16x32_bf16 v[132:135], v[28:31], a[140:143], 0\n"
      "ds_read_b128 a[184:187], v242 offset:5120\n"
      "v_cvt_pk_bf16_f32 v174, v140, v141\n"
      "v_mfma_f32_16x16x32_bf16 v[132:135], v[24:27], a[136:139], v[132:135]\n"
      ".Lst1_%=:\n"
      "s_cbranch_execz .Lstback_%=\n"
      "v_exp_f32_e32 v143, v143\n"
      "v_mfma_f32_16x16x32_bf16 v[132:135], v[20:23], a[132:135], v[132:135]\n"
      "ds_read_b128 a[188:191], v242 offset:7168\n"
      "v_cvt_pk_bf16_f32 v175, v142, v143\n"
      "v_mfma_f32_16x16x32_bf16 v[132:135], v[16:19], a[128:131], v[132:135]\n"
      "s_waitcnt lgkmcnt(4)\n"
      "s_barrier\n"
      "v_exp_f32_e32 v128, v128\n"
      "v_smfmac_f32_16x16x64_bf16 v[228:231], v[220:223], v[168:175], v241\n"
      "ds_write_b128 v242, v[144:147] offset:16384\n"
      "v_mfma_f32_16x16x32_bf16 a[224:227], a[0:3], a[160:163], 0\n"
      "s_add_u32 m0, s57, 0x3000\n"
      "s_mov_b32 s69, 0x7000\n"
      "buffer_load_dwordx4 v253, s[48:51], s69 offen offset:0 lds\n"
      "v_mfma_f32_16x16x32_bf16 a[224:227], a[8:11], a[164:167], a[224:227]\n"
      "v_exp_f32_e32 v129, v129\n"
      "v_mfma_f32_16x16x32_bf16 v[192:195], a[0:3], a[168:171], 0\n"
      "v_mfma_f32_16x16x32_bf16 v[192:195], a[8:11], a[172:175], v[192:195]\n"
      "v_exp_f32_e32 v130, v130\n"
      "v_mfma_f32_16x16x32_bf16 a[228:231], a[4:7], a[160:163], 0\n"
      "v_mfma_f32_16x16x32_bf16 a[228:231], a[12:15], a[164:167], a[228:231]\n"
      "v_exp_f32_e32 v131, v131\n"
      "v_mfma_f32_16x16x32_bf16 v[196:199], a[4:7], a[168:171], 0\n"
      "buffer_load_dwordx4 v253, s[48:51], s69 offen offset:1024 lds\n"
      "v_mfma_f32_16x16x32_bf16 v[196:199], a[12:15], a[172:175], v[196:199]\n"
      "s_waitcnt lgkmcnt(1)\n"
      "v_exp_f32_e32 v132, v132\n"
      "v_mfma_f32_16x16x32_bf16 v[136:139], v[0:3], a[144:147], 0\n"
      "ds_read_b128 a[160:163], v243 offset:0\n"
      "v_cvt_pk_bf16_f32 v144, v128, v129\n"
      "v_mfma_f32_16x16x32_bf16 v[136:139], v[4:7], a[148:151], v[136:139]\n"
      "v_exp_f32_e32 v133, v133\n"
      "v_mfma_f32_16x16x32_bf16 v[136:139], v[8:11], a[152:155], v[136:139]\n"
      "ds_read_b128 a[164:167], v243 offset:2048\n"
      "buffer_load_dwordx4 v253, s[48:51], s69 offen offset:2048 lds\n"
      "v_cvt_pk_bf16_f32 v145, v130, v131\n"
      "v_mfma_f32_16x16x32_bf16 v[136:139], v[12:15], a[156:159], v[136:139]\n"
      "v_exp_f32_e32 v134, v134\n"
      "v_mfma_f32_16x16x32_bf16 v[140:143], v[28:31], a[156:159], 0\n"
      "ds_read_b128 a[168:171], v243 offset:4096\n"
      "v_cvt_pk_bf16_f32 v146, v132, v133\n"
      "v_mfma_f32_16x16x32_bf16 v[140:143], v[24:27], a[152:155], v[140:143]\n"
      "v_exp_f32_e32 v135, v135\n"
      "v_mfma_f32_16x16x32_bf16 v[140:143], v[20:23], a[148:151], v[140:143]\n"
      "ds_read_b128 a[172:175], v243 offset:6144\n"
      "v_cvt_pk_bf16_f32 v147, v134, v135\n"
      "v_mfma_f32_16x16x32_bf16 v[140:143], v[16:19], a[144:147], v[140:143]\n"
      "buffer_load_dwordx4 v253, s[48:51], s69 offen offset:3072 lds\n"
      "v_exp_f32_e32 v136, v136\n"
      "v_mfma_f32_16x16x32_bf16 a[232:235], a[0:3], a[176:179], 0\n"
      "ds_write_b128 v242, v[160:163] offset:17408\n"
      "v_mfma_f32_16x16x32_bf16 a[232:235], a[8:11], a[180:183], a[232:235]\n"
      "v_exp_f32_e32 v137, v137\n"
      "v_mfma_f32_16x16x32_bf16 v[200:203], a[0:3], a[184:187], 0\n"
      "v_add_f32_e32 v235, v224, v225\n"
      "v_mfma_f32_16x16x32_bf16 v[200:203], a[8:11], a[188:191], v[200:203]\n"
      "v_exp_f32_e32 v138, v138\n"
      "v_mfma_f32_16x16x32_bf16 a[236:239], a[4:7], a[176:179], 0\n"
      "s_add_u32 m0, s60, 0x22000\n"
      "s_mov_b32 s58, 0xa000\n"
      "buffer_load_dwordx4 v248, s[40:43], s58 offen offset:0 lds\n"
      "v_add_f32_e32 v234, v228, v229\n"
      "v_mfma_f32_16x16x32_bf16 a[236:239], a[12:15], a[180:183], a[236:239]\n"
      "v_exp_f32_e32 v139, v139\n"
      "v_mfma_f32_16x16x32_bf16 v[204:207], a[4:7], a[184:187], 0\n"
      "v_mfma_f32_16x16x32_bf16 v[204:207], a[12:15], a[188:191], v[204:207]\n"
      "s_waitcnt lgkmcnt(1)\n"
      "v_exp_f32_e32 v140, v140\n"
      "v_mfma_f32_16x16x32_bf16 v[128:131], v[32:35], a[128:131], 0\n"
      "ds_read_b128 a[176:179], v243 offset:1024\n"
      "v_cvt_pk_bf16_f32 v160, v136, v137\n"
      "v_mfma_f32_16x16x32_bf16 v[128:131], v[36:39], a[132:135], v[128:131]\n"
      "buffer_load_dwordx4 v248, s[40:43], s58 offen offset:1024 lds\n"
      "v_exp_f32_e32 v141, v141\n"
      "v_mfma_f32_16x16x32_bf16 v[128:131], v[40:43], a[136:139], v[128:131]\n"
      "ds_read_b128 a[180:183], v243 offset:3072\n"
      "v_cvt_pk_bf16_f32 v161, v138, v139\n"
      "v_mfma_f32_16x16x32_bf16 v[128:131], v[44:47], a[140:143], v[128:131]\n"
      "v_exp_f32_e32 v142, v142\n"
      "v_mfma_f32_16x16x32_bf16 v[132:135], v[60:63], a[140:143], 0\n"
      "ds_read_b128 a[184:187], v243 offset:5120\n"
      "v_cvt_pk_bf16_f32 v162, v140, v141\n"
      "v_mfma_f32_16x16x32_bf16 v[132:135], v[56:59], a[136:139], v[132:135]\n"
      "v_exp_f32_e32 v143, v143\n"
      "v_mfma_f32_16x16x32_bf16 v[132:135], v[52:55], a[132:135], v[132:135]\n"
      "s_add_u32 m0, s60, 0x20000\n"
      "s_mov_b32 s58, 0x8000\n"
      "ds_read_b128 a[188:191], v243 offset:7168\n"
      "buffer_load_dwordx4 v248, s[40:43], s58 offen offset:0 lds\n"
      "v_cvt_pk_bf16_f32 v163, v142, v143\n"
      "v_mfma_f32_16x16x32_bf16 v[132:135], v[48:51], a[128:131], v[132:135]\n"
      "v_exp_f32_e32 v128, v128\n"
      "v_mfma_f32_16x16x32_bf16 a[224:227], a[32:35], a[160:163], a[224:227]\n"
      "ds_write_b128 v242, v[148:151] offset:18432\n"
      "v_mfma_f32_16x16x32_bf16 a[224:227], a[40:43], a[164:167], a[224:227]\n"
      "v_exp_f32_e32 v129, v129\n"
      "v_mfma_f32_16x16x32_bf16 v[192:195], a[32:35], a[168:171], v[192:195]\n"
      "v_mov_b32_e32 v224, 0\n"
      "v_mfma_f32_16x16x32_bf16 v[192:195], a[40:43], a[172:175], v[192:195]\n"
      "v_exp_f32_e32 v130, v130\n"
      "v_mfma_f32_16x16x32_bf16 a[228:231], a[36:39], a[160:163], a[228:231]\n"
      "buffer_load_dwordx4 v248, s[40:43], s58 offen offset:1024 lds\n"
      "v_mov_b32_e32 v225, 0\n"
      "v_mfma_f32_16x16x32_bf16 a[228:231], a[44:47], a[164:167], a[228:231]\n"
      "v_exp_f32_e32 v131, v131\n"
      "v_mfma_f32_16x16x32_bf16 v[196:199], a[36:39], a[168:171], v[196:199]\n"
      "v_mfma_f32_16x16x32_bf16 v[196:199], a[44:47], a[172:175], v[196:199]\n"
      "s_waitcnt lgkmcnt(1)\n"
      "v_exp_f32_e32 v132, v132\n"
      "v_mfma_f32_16x16x32_bf16 v[136:139], v[32:35], a[144:147], 0\n"
      "ds_read_b128 a[160:163], v244 offset:0\n"
      "v_cvt_pk_bf16_f32 v148, v128, v129\n"
      "v_mfma_f32_16x16x32_bf16 v[136:139], v[36:39], a[148:151], v[136:139]\n"
      "v_exp_f32_e32 v133, v133\n"
      "v_mfma_f32_16x16x32_bf16 v[136:139], v[40:43], a[152:155], v[136:139]\n"
      "ds_read_b128 a[164:167], v244 offset:2048\n"
      "v_cvt_pk_bf16_f32 v149, v130, v131\n"
      "v_mfma_f32_16x16x32_bf16 v[136:139], v[44:47], a[156:159], v[136:139]\n"
      "v_exp_f32_e32 v134, v134\n"
      "v_mfma_f32_16x16x32_bf16 v[140:143], v[60:63], a[156:159], 0\n"
      "ds_read_b128 a[168:171], v244 offset:4096\n"
      "v_cvt_pk_bf16_f32 v150, v132, v133\n"
      "v_mfma_f32_16x16x32_bf16 v[140:143], v[56:59], a[152:155], v[140:143]\n"
      "v_exp_f32_e32 v135, v135\n"
      "v_mfma_f32_16x16x32_bf16 v[140:143], v[52:55], a[148:151], v[140:143]\n"
      "ds_read_b128 a[172:175], v244 offset:6144\n"
      "v_cvt_pk_bf16_f32 v151, v134, v135\n"
      "v_mfma_f32_16x16x32_bf16 v[140:143], v[48:51], a[144:147], v[140:143]\n"
      "v_exp_f32_e32 v136, v136\n"
      "v_smfmac_f32_16x16x64_bf16 v[224:227], v[216:219], v[144:151], v241\n"
      "ds_write_b128 v242, v[164:167] offset:19456\n"
      "v_mfma_f32_16x16x32_bf16 a[232:235], a[32:35], a[176:179], a[232:235]\n"
      "v_mfma_f32_16x16x32_bf16 a[232:235], a[40:43], a[180:183], a[232:235]\n"
      "v_exp_f32_e32 v137, v137\n"
      "v_mfma_f32_16x16x32_bf16 v[200:203], a[32:35], a[184:187], v[200:203]\n"
      "v_mfma_f32_16x16x32_bf16 v[200:203], a[40:43], a[188:191], v[200:203]\n"
      "v_exp_f32_e32 v138, v138\n"
      "v_mfma_f32_16x16x32_bf16 a[236:239], a[36:39], a[176:179], a[236:239]\n"
      "v_mfma_f32_16x16x32_bf16 a[236:239], a[44:47], a[180:183], a[236:239]\n"
      "v_exp_f32_e32 v139, v139\n"
      "v_mfma_f32_16x16x32_bf16 v[204:207], a[36:39], a[184:187], v[204:207]\n"
      "v_mfma_f32_16x16x32_bf16 v[204:207], a[44:47], a[188:191], v[204:207]\n"
      "s_waitcnt lgkmcnt(1)\n"
      "v_exp_f32_e32 v140, v140\n"
      "v_mfma_f32_16x16x32_bf16 v[128:131], v[64:67], a[128:131], 0\n"
      "ds_read_b128 a[176:179], v244 offset:1024\n"
      "v_cvt_pk_bf16_f32 v164, v136, v137\n"
      "v_mfma_f32_16x16x32_bf16 v[128:131], v[68:71], a[132:135], v[128:131]\n"
      "v_exp_f32_e32 v141, v141\n"
      "v_mfma_f32_16x16x32_bf16 v[128:131], v[72:75], a[136:139], v[128:131]\n"
      "ds_read_b128 a[180:183], v244 offset:3072\n"
      "v_cvt_pk_bf16_f32 v165, v138, v139\n"
      "v_mfma_f32_16x16x32_bf16 v[128:131], v[76:79], a[140:143], v[128:131]\n"
      "v_exp_f32_e32 v142, v142\n"
      "v_mfma_f32_16x16x32_bf16 v[132:135], v[92:95], a[140:143], 0\n"
      "ds_read_b128 a[184:187], v244 offset:5120\n"
      "v_cvt_pk_bf16_f32 v166, v140, v141\n"
      "v_mfma_f32_16x16x32_bf16 v[132:135], v[88:91], a[136:139], v[132:135]\n"
      "v_exp_f32_e32 v143, v143\n"
      "v_mfma_f32_16x16x32_bf16 v[132:135], v[84:87], a[132:135], v[132:135]\n"
      "ds_read_b128 a[188:191], v244 offset:7168\n"
      "v_cvt_pk_bf16_f32 v167, v142, v143\n"
      "v_mfma_f32_16x16x32_bf16 v[132:135], v[80:83], a[128:131], v[132:135]\n"
      "v_exp_f32_e32 v128, v128\n"
      "v_smfmac_f32_16x16x64_bf16 v[224:227], v[220:223], v[160:167], v241\n"
      "ds_write_b128 v242, v[152:155] offset:20480\n"
      "v_mfma_f32_16x16x32_bf16 a[224:227], a[64:67], a[160:163], a[224:227]\n"
      "v_mfma_f32_16x16x32_bf16 a[224:227], a[72:75], a[164:167], a[224:227]\n"
      "v_exp_f32_e32 v129, v129\n"
      "v_mfma_f32_16x16x32_bf16 v[192:195], a[64:67], a[168:171], v[192:195]\n"
      "v_mfma_f32_16x16x32_bf16 v[192:195], a[72:75], a[172:175], v[192:195]\n"
      "v_exp_f32_e32 v130, v130\n"
      "v_mfma_f32_16x16x32_bf16 a[228:231], a[68:71], a[160:163], a[228:231]\n"
      "v_mfma_f32_16x16x32_bf16 a[228:231], a[76:79], a[164:167], a[228:231]\n"
      "v_exp_f32_e32 v131, v131\n"
      "v_mfma_f32_16x16x32_bf16 v[196:199], a[68:71], a[168:171], v[196:199]\n"
      "s_waitcnt vmcnt(12) lgkmcnt(15)\n"
      "s_barrier\n"
      "v_mfma_f32_16x16x32_bf16 v[196:199], a[76:79], a[172:175], v[196:199]\n"
      "s_waitcnt lgkmcnt(1)\n"
      "v_exp_f32_e32 v132, v132\n"
      "v_mfma_f32_16x16x32_bf16 v[136:139], v[64:67], a[144:147], 0\n"
      "ds_read_b128 a[160:163], v245 offset:0\n"
      "v_cvt_pk_bf16_f32 v152, v128, v129\n"
      "v_mfma_f32_16x16x32_bf16 v[136:139], v[68:71], a[148:151], v[136:139]\n"
      "ds_read_b64_tr_b16 a[16:17], v236 offset:24576\n"
      "v_exp_f32_e32 v133, v133\n"
      "v_mfma_f32_16x16x32_bf16 v[136:139], v[72:75], a[152:155], v[136:139]\n"
      "ds_read_b128 a[164:167], v245 offset:2048\n"
      "v_cvt_pk_bf16_f32 v153, v130, v131\n"
      "v_mfma_f32_16x16x32_bf16 v[136:139], v[76:79], a[156:159], v[136:139]\n"
      "ds_read_b64_tr_b16 a[18:19], v236 offset:28672\n"
      "v_exp_f32_e32 v134, v134\n"
      "v_mfma_f32_16x16x32_bf16 v[140:143], v[92:95], a[156:159], 0\n"
      "ds_read_b128 a[168:171], v245 offset:4096\n"
      "v_cvt_pk_bf16_f32 v154, v132, v133\n"
      "v_mfma_f32_16x16x32_bf16 v[140:143], v[88:91], a[152:155], v[140:143]\n"
      "v_exp_f32_e32 v135, v135\n"
      "v_mfma_f32_16x16x32_bf16 v[140:143], v[84:87], a[148:151], v[140:143]\n"
      "ds_read_b128 a[172:175], v245 offset:6144\n"
      "v_cvt_pk_bf16_f32 v155, v134, v135\n"
      "v_mfma_f32_16x16x32_bf16 v[140:143], v[80:83], a[144:147], v[140:143]\n"
      "ds_read_b64_tr_b16 a[20:21], v236 offset:24584\n"
      "v_exp_f32_e32 v136, v136\n"
      "v_mfma_f32_16x16x32_bf16 a[232:235], a[64:67], a[176:179], a[232:235]\n"
      "ds_write_b128 v242, v[168:171] offset:21504\n"
      "v_mfma_f32_16x16x32_bf16 a[232:235], a[72:75], a[180:183], a[232:235]\n"
      "v_exp_f32_e32 v137, v137\n"
      "v_mfma_f32_16x16x32_bf16 v[200:203], a[64:67], a[184:187], v[200:203]\n"
      "s_add_u32 m0, s60, 0x24000\n"
      "s_mov_b32 s58, 0xc000\n"
      "buffer_load_dwordx4 v248, s[40:43], s58 offen offset:0 lds\n"
      "v_mfma_f32_16x16x32_bf16 v[200:203], a[72:75], a[188:191], v[200:203]\n"
      "v_exp_f32_e32 v138, v138\n"
      "v_mfma_f32_16x16x32_bf16 a[236:239], a[68:71], a[176:179], a[236:239]\n"
      "ds_read_b64_tr_b16 a[22:23], v236 offset:28680\n"
      "v_mfma_f32_16x16x32_bf16 a[236:239], a[76:79], a[180:183], a[236:239]\n"
      "v_exp_f32_e32 v139, v139\n"
      "v_mfma_f32_16x16x32_bf16 v[204:207], a[68:71], a[184:187], v[204:207]\n"
      "buffer_load_dwordx4 v248, s[40:43], s58 offen offset:1024 lds\n"
      "v_mfma_f32_16x16x32_bf16 v[204:207], a[76:79], a[188:191], v[204:207]\n"
      "s_waitcnt lgkmcnt(3)\n"
      "ds_read_b64_tr_b16 a[48:49], v237 offset:24576\n"
      "v_exp_f32_e32 v140, v140\n"
      "v_mfma_f32_16x16x32_bf16 v[128:131], v[96:99], a[128:131], 0\n"
      "ds_read_b128 a[176:179], v245 offset:1024\n"
      "v_cvt_pk_bf16_f32 v168, v136, v137\n"
      "v_mfma_f32_16x16x32_bf16 v[128:131], v[100:103], a[132:135], v[128:131]\n"
      "v_exp_f32_e32 v141, v141\n"
      "v_mfma_f32_16x16x32_bf16 v[128:131], v[104:107], a[136:139], v[128:131]\n"
      "ds_read_b128 a[180:183], v245 offset:3072\n"
      "v_cvt_pk_bf16_f32 v169, v138, v139\n"
      "v_mfma_f32_16x16x32_bf16 v[128:131], v[108:111], a[140:143], v[128:131]\n"
      "ds_read_b64_tr_b16 a[50:51], v237 offset:28672\n"
      "v_exp_f32_e32 v142, v142\n"
      "v_mfma_f32_16x16x32_bf16 v[132:135], v[124:127], a[140:143], 0\n"
      "ds_read_b128 a[184:187], v245 offset:5120\n"
      "v_cvt_pk_bf16_f32 v170, v140, v141\n"
      "v_mfma_f32_16x16x32_bf16 v[132:135], v[120:123], a[136:139], v[132:135]\n"
      "v_exp_f32_e32 v143, v143\n"
      "v_mfma_f32_16x16x32_bf16 v[132:135], v[116:119], a[132:135], v[132:135]\n"
      "ds_read_b128 a[188:191], v245 offset:7168\n"
      "v_cvt_pk_bf16_f32 v171, v142, v143\n"
      "v_mfma_f32_16x16x32_bf16 v[132:135], v[112:115], a[128:131], v[132:135]\n"
      "ds_read_b64_tr_b16 a[52:53], v237 offset:24584\n"
      "v_exp_f32_e32 v128, v128\n"
      "v_mfma_f32_16x16x32_bf16 a[224:227], a[96:99], a[160:163], a[224:227]\n"
      "ds_write_b128 v242, v[156:159] offset:22528\n"
      "v_mfma_f32_16x16x32_bf16 a[224:227], a[104:107], a[164:167], a[224:227]\n"
      "v_exp_f32_e32 v129, v129\n"
      "v_mfma_f32_16x16x32_bf16 v[192:195], a[96:99], a[168:171], v[192:195]\n"
      "ds_read_b64_tr_b16 a[54:55], v237 offset:28680\n"
      "v_mfma_f32_16x16x32_bf16 v[192:195], a[104:107], a[172:175], v[192:195]\n"
      "v_exp_f32_e32 v130, v130\n"
      "v_mfma_f32_16x16x32_bf16 a[228:231], a[100:103], a[160:163], a[228:231]\n"
      "ds_read_b64_tr_b16 a[80:81], v238 offset:24576\n"
      "v_mfma_f32_16x16x32_bf16 a[228:231], a[108:111], a[164:167], a[228:231]\n"
      "v_exp_f32_e32 v131, v131\n"
      "v_mfma_f32_16x16x32_bf16 v[196:199], a[100:103], a[168:171], v[196:199]\n"
      "ds_read_b64_tr_b16 a[82:83], v238 offset:28672\n"
      "v_mfma_f32_16x16x32_bf16 v[196:199], a[108:111], a[172:175], v[196:199]\n"
      "s_waitcnt lgkmcnt(5)\n"
      "v_exp_f32_e32 v132, v132\n"
      "v_mfma_f32_16x16x32_bf16 v[136:139], v[96:99], a[144:147], 0\n"
      "ds_read_b128 a[128:131], v240 offset:8192\n"
      "v_cvt_pk_bf16_f32 v156, v128, v129\n"
      "v_mfma_f32_16x16x32_bf16 v[136:139], v[100:103], a[148:151], v[136:139]\n"
      "ds_read_b64_tr_b16 a[84:85], v238 offset:24584\n"
      "v_exp_f32_e32 v133, v133\n"
      "v_mfma_f32_16x16x32_bf16 v[136:139], v[104:107], a[152:155], v[136:139]\n"
      "ds_read_b128 a[132:135], v240 offset:8448\n"
      "v_cvt_pk_bf16_f32 v157, v130, v131\n"
      "v_mfma_f32_16x16x32_bf16 v[136:139], v[108:111], a[156:159], v[136:139]\n"
      "v_exp_f32_e32 v134, v134\n"
      "v_mfma_f32_16x16x32_bf16 v[140:143], v[124:127], a[156:159], 0\n"
      "ds_read_b128 a[136:139], v240 offset:8704\n"
      "v_cvt_pk_bf16_f32 v158, v132, v133\n"
      "v_mfma_f32_16x16x32_bf16 v[140:143], v[120:123], a[152:155], v[140:143]\n"
      "ds_read_b64_tr_b16 a[86:87], v238 offset:28680\n"
      "v_exp_f32_e32 v135, v135\n"
      "v_mfma_f32_16x16x32_bf16 v[140:143], v[116:119], a[148:151], v[140:143]\n"
      "ds_read_b128 a[140:143], v240 offset:8960\n"
      "v_cvt_pk_bf16_f32 v159, v134, v135\n"
      "v_mfma_f32_16x16x32_bf16 v[140:143], v[112:115], a[144:147], v[140:143]\n"
      "v_exp_f32_e32 v136, v136\n"
      "v_smfmac_f32_16x16x64_bf16 v[224:227], v[216:219], v[152:159], v241\n"
      "ds_write_b128 v242, v[172:175] offset:23552\n"
      "v_mfma_f32_16x16x32_bf16 a[232:235], a[96:99], a[176:179], a[232:235]\n"
      "v_mfma_f32_16x16x32_bf16 a[232:235], a[104:107], a[180:183], a[232:235]\n"
      "v_exp_f32_e32 v137, v137\n"
      "v_mfma_f32_16x16x32_bf16 v[200:203], a[96:99], a[184:187], v[200:203]\n"
      "ds_read_b64_tr_b16 a[112:113], v239 offset:24576\n"
      "ds_read_b64_tr_b16 a[114:115], v239 offset:28672\n"
      "v_mfma_f32_16x16x32_bf16 v[200:203], a[104:107], a[188:191], v[200:203]\n"
      "v_exp_f32_e32 v138, v138\n"
      "v_mfma_f32_16x16x32_bf16 a[236:239], a[100:103], a[176:179], a[236:239]\n"
      "ds_read_b64_tr_b16 a[116:117], v239 offset:24584\n"
      "v_mfma_f32_16x16x32_bf16 a[236:239], a[108:111], a[180:183], a[236:239]\n"
      "v_exp_f32_e32 v139, v139\n"
      "v_mfma_f32_16x16x32_bf16 v[204:207], a[100:103], a[184:187], v[204:207]\n"
      "ds_read_b64_tr_b16 a[118:119], v239 offset:28680\n"
      "v_mfma_f32_16x16x32_bf16 v[204:207], a[108:111], a[188:191], v[204:207]\n"
      "s_waitcnt lgkmcnt(5)\n"
      "v_exp_f32_e32 v140, v140\n"
      "s_waitcnt lgkmcnt(0)\n"
      "s_barrier\n"
      "ds_read_b128 a[160:163], v242 offset:16384\n"
      "ds_read_b128 a[164:167], v242 offset:17408\n"
      "ds_read_b128 a[168:171], v242 offset:20480\n"
      "ds_read_b128 a[172:175], v242 offset:21504\n"
      "ds_read_b128 a[176:179], v243 offset:16384\n"
      "ds_read_b128 a[180:183], v243 offset:17408\n"
      "ds_read_b128 a[184:187], v243 offset:20480\n"
      "ds_read_b128 a[188:191], v243 offset:21504\n"
      "s_waitcnt lgkmcnt(4)\n"
      "v_mfma_f32_16x16x32_bf16 a[224:227], a[16:19], a[160:163], a[224:227]\n"
      "ds_read_b128 a[192:195], v244 offset:16384\n"
      "v_mfma_f32_16x16x32_bf16 a[228:231], a[20:23], a[160:163], a[228:231]\n"
      "ds_read_b128 a[196:199], v244 offset:17408\n"
      "v_mfma_f32_16x16x32_bf16 a[232:235], a[16:19], a[164:167], a[232:235]\n"
      "ds_read_b128 a[200:203], v244 offset:20480\n"
      "v_mfma_f32_16x16x32_bf16 a[236:239], a[20:23], a[164:167], a[236:239]\n"
      "ds_read_b128 a[204:207], v244 offset:21504\n"
      "v_mfma_f32_16x16x32_bf16 v[192:195], a[16:19], a[168:171], v[192:195]\n"
      "v_mfma_f32_16x16x32_bf16 v[196:199], a[20:23], a[168:171], v[196:199]\n"
      "ds_write_b128 v242, v[144:147] offset:0\n"
      "v_mfma_f32_16x16x32_bf16 v[200:203], a[16:19], a[172:175], v[200:203]\n"
      "v_mfma_f32_16x16x32_bf16 v[204:207], a[20:23], a[172:175], v[204:207]\n"
      "s_waitcnt lgkmcnt(5)\n"
      "v_mfma_f32_16x16x32_bf16 a[224:227], a[48:51], a[176:179], a[224:227]\n"
      "ds_read_b128 a[208:211], v245 offset:16384\n"
      "v_mfma_f32_16x16x32_bf16 a[228:231], a[52:55], a[176:179], a[228:231]\n"
      "ds_read_b128 a[212:215], v245 offset:17408\n"
      "v_mfma_f32_16x16x32_bf16 a[232:235], a[48:51], a[180:183], a[232:235]\n"
      "ds_read_b128 a[216:219], v245 offset:20480\n"
      "v_mfma_f32_16x16x32_bf16 a[236:239], a[52:55], a[180:183], a[236:239]\n"
      "ds_read_b128 a[220:223], v245 offset:21504\n"
      "v_mfma_f32_16x16x32_bf16 v[192:195], a[48:51], a[184:187], v[192:195]\n"
      "ds_write_b32 v246, v235 offset:2048\n"
      "v_mfma_f32_16x16x32_bf16 v[196:199], a[52:55], a[184:187], v[196:199]\n"
      "ds_write_b32 v246, v234 offset:3072\n"
      "v_mfma_f32_16x16x32_bf16 v[200:203], a[48:51], a[188:191], v[200:203]\n"
      "s_waitcnt vmcnt(6)\n"
      "s_barrier\n"
      "v_mfma_f32_16x16x32_bf16 v[204:207], a[52:55], a[188:191], v[204:207]\n"
      "s_waitcnt lgkmcnt(7)\n"
      "ds_read_b64_tr_b16 a[24:25], v236 offset:8192\n"
      "v_mfma_f32_16x16x32_bf16 a[224:227], a[80:83], a[192:195], a[224:227]\n"
      "ds_read_b128 a[160:163], v242 offset:18432\n"
      "ds_read_b64_tr_b16 a[26:27], v236 offset:12288\n"
      "v_mfma_f32_16x16x32_bf16 a[228:231], a[84:87], a[192:195], a[228:231]\n"
      "ds_read_b128 a[164:167], v242 offset:19456\n"
      "v_mfma_f32_16x16x32_bf16 a[232:235], a[80:83], a[196:199], a[232:235]\n"
      "ds_read_b128 a[168:171], v242 offset:22528\n"
      "ds_read_b64_tr_b16 a[28:29], v236 offset:8200\n"
      "v_mfma_f32_16x16x32_bf16 a[236:239], a[84:87], a[196:199], a[236:239]\n"
      "ds_read_b128 a[172:175], v242 offset:23552\n"
      "v_mfma_f32_16x16x32_bf16 v[192:195], a[80:83], a[200:203], v[192:195]\n"
      "ds_read_b64_tr_b16 a[30:31], v236 offset:12296\n"
      "v_mfma_f32_16x16x32_bf16 v[196:199], a[84:87], a[200:203], v[196:199]\n"
      "ds_read_b64_tr_b16 a[56:57], v237 offset:8192\n"
      "v_mfma_f32_16x16x32_bf16 v[200:203], a[80:83], a[204:207], v[200:203]\n"
      "ds_read_b64_tr_b16 a[58:59], v237 offset:12288\n"
      "v_mfma_f32_16x16x32_bf16 v[204:207], a[84:87], a[204:207], v[204:207]\n"
      "s_waitcnt lgkmcnt(12)\n"
      "v_mfma_f32_16x16x32_bf16 a[224:227], a[112:115], a[208:211], a[224:227]\n"
      "ds_read_b128 a[176:179], v243 offset:18432\n"
      "ds_read_b64_tr_b16 a[60:61], v237 offset:8200\n"
      "v_mfma_f32_16x16x32_bf16 a[228:231], a[116:119], a[208:211], a[228:231]\n"
      "ds_read_b128 a[180:183], v243 offset:19456\n"
      "v_mfma_f32_16x16x32_bf16 a[232:235], a[112:115], a[212:215], a[232:235]\n"
      "ds_read_b128 a[184:187], v243 offset:22528\n"
      "ds_read_b64_tr_b16 a[62:63], v237 offset:12296\n"
      "v_mfma_f32_16x16x32_bf16 a[236:239], a[116:119], a[212:215], a[236:239]\n"
      "ds_read_b128 a[188:191], v243 offset:23552\n"
      "v_mfma_f32_16x16x32_bf16 v[192:195], a[112:115], a[216:219], v[192:195]\n"
      "ds_read_b64_tr_b16 a[88:89], v238 offset:8192\n"
      "v_mfma_f32_16x16x32_bf16 v[196:199], a[116:119], a[216:219], v[196:199]\n"
      "ds_read_b64_tr_b16 a[90:91], v238 offset:12288\n"
      "v_mfma_f32_16x16x32_bf16 v[200:203], a[112:115], a[220:223], v[200:203]\n"
      "ds_write_b128 v242, v[160:163] offset:1024\n"
      "v_mfma_f32_16x16x32_bf16 v[204:207], a[116:119], a[220:223], v[204:207]\n"
      "s_waitcnt lgkmcnt(11)\n"
      "v_mfma_f32_16x16x32_bf16 a[224:227], a[24:27], a[160:163], a[224:227]\n"
      "ds_read_b128 a[192:195], v244 offset:18432\n"
      "v_mfma_f32_16x16x32_bf16 a[228:231], a[28:31], a[160:163], a[228:231]\n"
      "ds_read_b128 a[196:199], v244 offset:19456\n"
      "v_mfma_f32_16x16x32_bf16 a[232:235], a[24:27], a[164:167], a[232:235]\n"
      "ds_read_b128 a[200:203], v244 offset:22528\n"
      "v_mfma_f32_16x16x32_bf16 a[236:239], a[28:31], a[164:167], a[236:239]\n"
      "ds_read_b128 a[204:207], v244 offset:23552\n"
      "v_mfma_f32_16x16x32_bf16 v[192:195], a[24:27], a[168:171], v[192:195]\n"
      "ds_read_b64_tr_b16 a[92:93], v238 offset:8200\n"
      "v_mfma_f32_16x16x32_bf16 v[196:199], a[28:31], a[168:171], v[196:199]\n"
      "ds_read_b64_tr_b16 a[94:95], v238 offset:12296\n"
      "v_mfma_f32_16x16x32_bf16 v[200:203], a[24:27], a[172:175], v[200:203]\n"
      "ds_write_b128 v242, v[148:151] offset:2048\n"
      "v_mfma_f32_16x16x32_bf16 v[204:207], a[28:31], a[172:175], v[204:207]\n"
      "s_waitcnt lgkmcnt(10)\n"
      "v_mfma_f32_16x16x32_bf16 a[224:227], a[56:59], a[176:179], a[224:227]\n"
      "ds_read_b128 a[208:211], v245 offset:18432\n"
      "v_mfma_f32_16x16x32_bf16 a[228:231], a[60:63], a[176:179], a[228:231]\n"
      "ds_read_b128 a[212:215], v245 offset:19456\n"
      "v_mfma_f32_16x16x32_bf16 a[232:235], a[56:59], a[180:183], a[232:235]\n"
      "ds_read_b128 a[216:219], v245 offset:22528\n"
      "v_mfma_f32_16x16x32_bf16 a[236:239], a[60:63], a[180:183], a[236:239]\n"
      "ds_read_b128 a[220:223], v245 offset:23552\n"
      "v_mfma_f32_16x16x32_bf16 v[192:195], a[56:59], a[184:187], v[192:195]\n"
      "ds_read_b64_tr_b16 a[120:121], v239 offset:8192\n"
      "v_mfma_f32_16x16x32_bf16 v[196:199], a[60:63], a[184:187], v[196:199]\n"
      "v_mfma_f32_16x16x32_bf16 v[200:203], a[56:59], a[188:191], v[200:203]\n"
      "ds_read_b64_tr_b16 a[122:123], v239 offset:12288\n"
      "v_mfma_f32_16x16x32_bf16 v[204:207], a[60:63], a[188:191], v[204:207]\n"
      "s_waitcnt lgkmcnt(7)\n"
      "v_mfma_f32_16x16x32_bf16 a[224:227], a[88:91], a[192:195], a[224:227]\n"
      "ds_read_b64_tr_b16 a[124:125], v239 offset:8200\n"
      "v_mfma_f32_16x16x32_bf16 a[228:231], a[92:95], a[192:195], a[228:231]\n"
      "v_mfma_f32_16x16x32_bf16 a[232:235], a[88:91], a[196:199], a[232:235]\n"
      "ds_read_b64_tr_b16 a[126:127], v239 offset:12296\n"
      "v_mfma_f32_16x16x32_bf16 a[236:239], a[92:95], a[196:199], a[236:239]\n"
      "v_mfma_f32_16x16x32_bf16 v[192:195], a[88:91], a[200:203], v[192:195]\n"
      "v_mfma_f32_16x16x32_bf16 v[196:199], a[92:95], a[200:203], v[196:199]\n"
      "ds_write_b128 v242, v[164:167] offset:3072\n"
      "v_mfma_f32_16x16x32_bf16 v[200:203], a[88:91], a[204:207], v[200:203]\n"
      "v_mfma_f32_16x16x32_bf16 v[204:207], a[92:95], a[204:207], v[204:207]\n"
      "s_waitcnt lgkmcnt(1)\n"
      "v_mfma_f32_16x16x32_bf16 a[224:227], a[120:123], a[208:211], a[224:227]\n"
      "ds_write_b128 v242, v[152:155] offset:4096\n"
      "v_mfma_f32_16x16x32_bf16 a[228:231], a[124:127], a[208:211], a[228:231]\n"
      "v_mfma_f32_16x16x32_bf16 a[232:235], a[120:123], a[212:215], a[232:235]\n"
      "v_mfma_f32_16x16x32_bf16 a[236:239], a[124:127], a[212:215], a[236:239]\n"
      "ds_write_b128 v242, v[168:171] offset:5120\n"
      "v_mfma_f32_16x16x32_bf16 v[192:195], a[120:123], a[216:219], v[192:195]\n"
      "v_mfma_f32_16x16x32_bf16 v[196:199], a[124:127], a[216:219], v[196:199]\n"
      "v_mfma_f32_16x16x32_bf16 v[200:203], a[120:123], a[220:223], v[200:203]\n"
      "ds_write_b128 v242, v[156:159] offset:6144\n"
      "v_mfma_f32_16x16x32_bf16 v[204:207], a[124:127], a[220:223], v[204:207]\n"
      "v_mfma_f32_16x16x32_bf16 v[128:131], v[0:3], a[128:131], 0\n"
      "ds_read_b128 a[144:147], v240 offset:12288\n"
      "v_cvt_pk_bf16_f32 v172, v136, v137\n"
      "v_mfma_f32_16x16x32_bf16 v[128:131], v[4:7], a[132:135], v[128:131]\n"
      "v_exp_f32_e32 v141, v141\n"
      "v_mfma_f32_16x16x32_bf16 v[128:131], v[8:11], a[136:139], v[128:131]\n"
      "ds_read_b128 a[148:151], v240 offset:12544\n"
      "v_cvt_pk_bf16_f32 v173, v138, v139\n"
      "v_mfma_f32_16x16x32_bf16 v[128:131], v[12:15], a[140:143], v[128:131]\n"
      "v_exp_f32_e32 v142, v142\n"
      "v_mfma_f32_16x16x32_bf16 v[132:135], v[28:31], a[140:143], 0\n"
      "ds_read_b128 a[152:155], v240 offset:12800\n"
      "v_cvt_pk_bf16_f32 v174, v140, v141\n"
      "v_mfma_f32_16x16x32_bf16 v[132:135], v[24:27], a[136:139], v[132:135]\n"
      "v_exp_f32_e32 v143, v143\n"
      "v_mfma_f32_16x16x32_bf16 v[132:135], v[20:23], a[132:135], v[132:135]\n"
      "ds_read_b128 a[156:159], v240 offset:13056\n"
      "v_cvt_pk_bf16_f32 v175, v142, v143\n"
      "v_mfma_f32_16x16x32_bf16 v[132:135], v[16:19], a[128:131], v[132:135]\n"
      "v_exp_f32_e32 v128, v128\n"
      "v_smfmac_f32_16x16x64_bf16 v[224:227], v[220:223], v[168:175], v241\n"
      "ds_write_b128 v242, v[172:175] offset:7168\n"
      "v_mfma_f32_16x16x32_bf16 v[176:179], a[0:3], v[144:147], 0\n"
      "v_xor_b32_e32 v240, 0x4000, v240\n"
      "v_mfma_f32_16x16x32_bf16 v[176:179], a[8:11], v[148:151], v[176:179]\n"
      "v_exp_f32_e32 v129, v129\n"
      "v_mfma_f32_16x16x32_bf16 v[176:179], a[16:19], v[152:155], v[176:179]\n"
      "v_mfma_f32_16x16x32_bf16 v[176:179], a[24:27], v[156:159], v[176:179]\n"
      "v_exp_f32_e32 v130, v130\n"
      "v_mfma_f32_16x16x32_bf16 v[180:183], a[28:31], v[156:159], 0\n"
      "v_add_f32_e32 v235, v224, v225\n"
      "v_mov_b32_e32 v224, 0\n"
      "v_mfma_f32_16x16x32_bf16 v[180:183], a[20:23], v[152:155], v[180:183]\n"
      "v_exp_f32_e32 v131, v131\n"
      "v_mfma_f32_16x16x32_bf16 v[180:183], a[12:15], v[148:151], v[180:183]\n"
      "s_waitcnt vmcnt(2) lgkmcnt(0)\n"
      "s_barrier\n"
      "v_mfma_f32_16x16x32_bf16 v[180:183], a[4:7], v[144:147], v[180:183]\n"
      "v_exp_f32_e32 v132, v132\n"
      "v_mfma_f32_16x16x32_bf16 v[136:139], v[0:3], a[144:147], 0\n"
      "ds_read_b128 a[160:163], v243 offset:0\n"
      "ds_read_b128 v[228:231], v247 offset:2048\n"
      "v_mfma_f32_16x16x32_bf16 a[240:243], a[8:11], a[8:11], a[240:243]\n"
      "v_cvt_pk_bf16_f32 v144, v128, v129\n"
      "v_mfma_f32_16x16x32_bf16 v[136:139], v[4:7], a[148:151], v[136:139]\n"
      "v_exp_f32_e32 v133, v133\n"
      "v_mfma_f32_16x16x32_bf16 v[136:139], v[8:11], a[152:155], v[136:139]\n"
      "ds_read_b128 a[164:167], v243 offset:2048\n"
      "v_cvt_pk_bf16_f32 v145, v130, v131\n"
      "v_mfma_f32_16x16x32_bf16 v[136:139], v[12:15], a[156:159], v[136:139]\n"
      "v_exp_f32_e32 v134, v134\n"
      "v_mfma_f32_16x16x32_bf16 v[140:143], v[28:31], a[156:159], 0\n"
      "ds_read_b128 a[168:171], v243 offset:4096\n"
      "v_cvt_pk_bf16_f32 v146, v132, v133\n"
      "v_mfma_f32_16x16x32_bf16 v[140:143], v[24:27], a[152:155], v[140:143]\n"
      "v_exp_f32_e32 v135, v135\n"
      "v_mfma_f32_16x16x32_bf16 v[140:143], v[20:23], a[148:151], v[140:143]\n"
      "ds_read_b128 a[172:175], v243 offset:6144\n"
      "s_waitcnt lgkmcnt(3)\n"
      "v_mfma_f32_16x16x32_bf16 a[240:243], a[12:15], a[12:15], a[240:243]\n"
      "v_cvt_pk_bf16_f32 v147, v134, v135\n"
      "v_mfma_f32_16x16x32_bf16 v[140:143], v[16:19], a[144:147], v[140:143]\n"
      "v_exp_f32_e32 v136, v136\n"
      "v_mfma_f32_16x16x32_bf16 a[240:243], a[40:43], a[40:43], a[240:243]\n"
      "v_add_f32_e32 v228, v228, v229\n"
      "v_mfma_f32_16x16x32_bf16 v[184:187], a[0:3], v[160:163], 0\n"
      "ds_write_b128 v242, v[144:147] offset:8192\n"
      "v_mfma_f32_16x16x32_bf16 v[184:187], a[8:11], v[164:167], v[184:187]\n"
      "v_exp_f32_e32 v137, v137\n"
      "v_mfma_f32_16x16x32_bf16 a[240:243], a[44:47], a[44:47], a[240:243]\n"
      "v_add_f32_e32 v230, v230, v231\n"
      "v_mfma_f32_16x16x32_bf16 v[184:187], a[16:19], v[168:171], v[184:187]\n"
      "v_mov_b32_e32 v225, 0\n"
      "v_add_f32_e32 v234, v228, v230\n"
      "v_mfma_f32_16x16x32_bf16 v[184:187], a[24:27], v[172:175], v[184:187]\n"
      "s_mov_b32 s59, 0\n"
      "v_exp_f32_e32 v138, v138\n"
      "v_mfma_f32_16x16x32_bf16 a[240:243], a[72:75], a[72:75], a[240:243]\n"
      "v_rcp_f32_e32 v232, v234\n"
      "v_mfma_f32_16x16x32_bf16 v[188:191], a[28:31], v[172:175], 0\n"
      "v_mov_b32_e32 v233, v232\n"
      "v_mfma_f32_16x16x32_bf16 v[188:191], a[20:23], v[168:171], v[188:191]\n"
      "v_exp_f32_e32 v139, v139\n"
      "v_mfma_f32_16x16x32_bf16 v[188:191], a[12:15], v[164:167], v[188:191]\n"
      "ds_write_b32 v246, v235 offset:0\n"
      "v_med3_f32 v229, v234, 1.0, s63\n"
      "v_mfma_f32_16x16x32_bf16 v[188:191], a[4:7], v[160:163], v[188:191]\n"
      "s_waitcnt lgkmcnt(2)\n"
      "v_exp_f32_e32 v140, v140\n"
      "v_mfma_f32_16x16x32_bf16 a[240:243], a[76:79], a[76:79], a[240:243]\n"
      "v_cmp_neq_f32_e32 vcc, v229, v234\n"
      "v_mfma_f32_16x16x32_bf16 v[128:131], v[32:35], a[128:131], 0\n"
      "ds_read_b128 a[176:179], v243 offset:1024\n"
      "v_cvt_pk_bf16_f32 v160, v136, v137\n"
      "v_mfma_f32_16x16x32_bf16 a[240:243], a[104:107], a[104:107], a[240:243]\n"
      "v_permlane32_swap_b32_e32 v232, v233\n"
      "v_mfma_f32_16x16x32_bf16 v[128:131], v[36:39], a[132:135], v[128:131]\n"
      "s_cmp_lg_u64 vcc, 0\n"
      "s_addc_u32 s66, s66, s66\n"
      "v_mfma_f32_16x16x32_bf16 a[240:243], a[108:111], a[108:111], a[240:243]\n"
      "v_exp_f32_e32 v141, v141\n"
      "v_mfma_f32_16x16x32_bf16 v[128:131], v[40:43], a[136:139], v[128:131]\n"
      "ds_read_b128 a[180:183], v243 offset:3072\n"
      "v_cvt_pk_bf16_f32 v161, v138, v139\n"
      "v_mfma_f32_16x16x32_bf16 v[128:131], v[44:47], a[140:143], v[128:131]\n"
      "v_exp_f32_e32 v142, v142\n"
      "v_mfma_f32_16x16x32_bf16 a[240:243], a[16:19], a[16:19], a[240:243]\n"
      "v_accvgpr_read_b32 v236, a224\n"
      "v_mfma_f32_16x16x32_bf16 v[132:135], v[60:63], a[140:143], 0\n"
      "ds_read_b128 a[184:187], v243 offset:5120\n"
      "v_mfma_f32_16x16x32_bf16 a[240:243], a[20:23], a[20:23], a[240:243]\n"
      "v_cvt_pk_bf16_f32 v162, v140, v141\n"
      "v_accvgpr_read_b32 v237, a225\n"
      "v_mfma_f32_16x16x32_bf16 v[132:135], v[56:59], a[136:139], v[132:135]\n"
      "v_exp_f32_e32 v143, v143\n"
      "v_mfma_f32_16x16x32_bf16 a[240:243], a[48:51], a[48:51], a[240:243]\n"
      "v_accvgpr_read_b32 v238, a226\n"
      "v_mfma_f32_16x16x32_bf16 v[132:135], v[52:55], a[132:135], v[132:135]\n"
      "ds_read_b128 a[188:191], v243 offset:7168\n"
      "v_mfma_f32_16x16x32_bf16 a[240:243], a[52:55], a[52:55], a[240:243]\n"
      "v_cvt_pk_bf16_f32 v163, v142, v143\n"
      "v_accvgpr_read_b32 v239, a227\n"
      "v_mfma_f32_16x16x32_bf16 v[132:135], v[48:51], a[128:131], v[132:135]\n"
      "v_exp_f32_e32 v128, v128\n"
      "v_mfma_f32_16x16x32_bf16 a[240:243], a[80:83], a[80:83], a[240:243]\n"
      "v_mul_f32_e32 v236, v232, v236\n"
      "v_mfma_f32_16x16x32_bf16 v[176:179], a[32:35], a[160:163], v[176:179]\n"
      "ds_write_b128 v242, v[160:163] offset:9216\n"
      "v_mfma_f32_16x16x32_bf16 v[176:179], a[40:43], a[164:167], v[176:179]\n"
      "v_exp_f32_e32 v129, v129\n"
      "v_mfma_f32_16x16x32_bf16 a[240:243], a[84:87], a[84:87], a[240:243]\n"
      "v_mul_f32_e32 v237, v232, v237\n"
      "v_mfma_f32_16x16x32_bf16 v[176:179], a[48:51], a[168:171], v[176:179]\n"
      "s_add_u32 m0, s60, 0x26000\n"
      "s_mov_b32 s58, 0xe000\n"
      "buffer_load_dwordx4 v248, s[40:43], s58 offen offset:0 lds\n"
      "v_mfma_f32_16x16x32_bf16 a[240:243], a[112:115], a[112:115], a[240:243]\n"
      "v_mul_f32_e32 v238, v232, v238\n"
      "v_mfma_f32_16x16x32_bf16 v[176:179], a[56:59], a[172:175], v[176:179]\n"
      ".Lst2_%=:\n"
      "s_cbranch_execz .Lstback_%=\n"
      "v_exp_f32_e32 v130, v130\n"
      "v_mfma_f32_16x16x32_bf16 a[240:243], a[116:119], a[116:119], a[240:243]\n"
      "v_mul_f32_e32 v239, v232, v239\n"
      "v_mfma_f32_16x16x32_bf16 v[180:183], a[60:63], a[172:175], v[180:183]\n"
      "v_cvt_pk_bf16_f32 v208, v236, v237\n"
      "v_mfma_f32_16x16x32_bf16 a[240:243], a[24:27], a[24:27], a[240:243]\n"
      "v_cvt_pk_bf16_f32 v209, v238, v239\n"
      "v_accvgpr_read_b32 v236, a228\n"
      "v_mfma_f32_16x16x32_bf16 v[180:183], a[52:55], a[168:171], v[180:183]\n"
      "v_exp_f32_e32 v131, v131\n"
      "v_mfma_f32_16x16x32_bf16 a[240:243], a[28:31], a[28:31], a[240:243]\n"
      "v_accvgpr_read_b32 v237, a229\n"
      "v_mfma_f32_16x16x32_bf16 v[180:183], a[44:47], a[164:167], v[180:183]\n"
      "v_accvgpr_read_b32 v238, a230\n"
      "v_mfma_f32_16x16x32_bf16 a[240:243], a[56:59], a[56:59], a[240:243]\n"
      "v_accvgpr_read_b32 v239, a231\n"
      "v_mul_f32_e32 v236, v232, v236\n"
      "v_mfma_f32_16x16x32_bf16 v[180:183], a[36:39], a[160:163], v[180:183]\n"
      "s_waitcnt lgkmcnt(1)\n"
      "v_exp_f32_e32 v132, v132\n"
      "v_mfma_f32_16x16x32_bf16 a[240:243], a[60:63], a[60:63], a[240:243]\n"
      "v_mul_f32_e32 v237, v232, v237\n"
      "v_mfma_f32_16x16x32_bf16 v[136:139], v[32:35], a[144:147], 0\n"
      "ds_read_b128 a[160:163], v244 offset:0\n"
      "v_mfma_f32_16x16x32_bf16 a[240:243], a[88:91], a[88:91], a[240:243]\n"
      "v_cvt_pk_bf16_f32 v148, v128, v129\n"
      "v_mul_f32_e32 v238, v232, v238\n"
      "v_mfma_f32_16x16x32_bf16 v[136:139], v[36:39], a[148:151], v[136:139]\n"
      "v_exp_f32_e32 v133, v133\n"
      "v_mfma_f32_16x16x32_bf16 a[240:243], a[92:95], a[92:95], a[240:243]\n"
      "v_mul_f32_e32 v239, v232, v239\n"
      "v_mfma_f32_16x16x32_bf16 v[136:139], v[40:43], a[152:155], v[136:139]\n"
      "ds_read_b128 a[164:167], v244 offset:2048\n"
      "v_cvt_pk_bf16_f32 v149, v130, v131\n"
      "v_cvt_pk_bf16_f32 v210, v236, v237\n"
      "v_mfma_f32_16x16x32_bf16 v[136:139], v[44:47], a[156:159], v[136:139]\n"
      "v_exp_f32_e32 v134, v134\n"
      "v_cvt_pk_bf16_f32 v211, v238, v239\n"
      "v_mfma_f32_16x16x32_bf16 v[140:143], v[60:63], a[156:159], 0\n"
      "ds_read_b128 a[168:171], v244 offset:4096\n"
      "v_cvt_pk_bf16_f32 v150, v132, v133\n"
      "v_mfma_f32_16x16x32_bf16 v[140:143], v[56:59], a[152:155], v[140:143]\n"
      "v_exp_f32_e32 v135, v135\n"
      "v_mfma_f32_16x16x32_bf16 v[140:143], v[52:55], a[148:151], v[140:143]\n"
      "ds_read_b128 a[172:175], v244 offset:6144\n"
      "v_cvt_pk_bf16_f32 v151, v134, v135\n"
      "v_mfma_f32_16x16x32_bf16 v[140:143], v[48:51], a[144:147], v[140:143]\n"
      "v_exp_f32_e32 v136, v136\n"
      "v_smfmac_f32_16x16x64_bf16 v[224:227], v[216:219], v[144:151], v241\n"
      "ds_write_b128 v242, v[148:151] offset:10240\n"
      "v_mfma_f32_16x16x32_bf16 v[184:187], a[32:35], a[176:179], v[184:187]\n"
      "buffer_store_dwordx4 v[208:211], v249, s[52:55], s59 offen offset:0 sc0 sc1\n"
      "v_mfma_f32_16x16x32_bf16 v[184:187], a[40:43], a[180:183], v[184:187]\n"
      "v_exp_f32_e32 v137, v137\n"
      "v_accvgpr_read_b32 v236, a232\n"
      "v_mfma_f32_16x16x32_bf16 v[184:187], a[48:51], a[184:187], v[184:187]\n"
      "buffer_load_dwordx4 v248, s[40:43], s58 offen offset:1024 lds\n"
      "v_accvgpr_read_b32 v237, a233\n"
      "v_accvgpr_read_b32 v238, a234\n"
      "v_mfma_f32_16x16x32_bf16 v[184:187], a[56:59], a[188:191], v[184:187]\n"
      "v_exp_f32_e32 v138, v138\n"
      "v_accvgpr_read_b32 v239, a235\n"
      "v_mfma_f32_16x16x32_bf16 v[188:191], a[60:63], a[188:191], v[188:191]\n"
      "v_mul_f32_e32 v236, v233, v236\n"
      "v_mul_f32_e32 v237, v233, v237\n"
      "v_mul_f32_e32 v238, v233, v238\n"
      "v_mfma_f32_16x16x32_bf16 v[188:191], a[52:55], a[184:187], v[188:191]\n"
      "v_exp_f32_e32 v139, v139\n"
      "v_mul_f32_e32 v239, v233, v239\n"
      "v_mfma_f32_16x16x32_bf16 v[188:191], a[44:47], a[180:183], v[188:191]\n"
      "v_cvt_pk_bf16_f32 v212, v236, v237\n"
      "v_cvt_pk_bf16_f32 v213, v238, v239\n"
      "v_accvgpr_read_b32 v236, a236\n"
      "v_mfma_f32_16x16x32_bf16 v[188:191], a[36:39], a[176:179], v[188:191]\n"
      "s_waitcnt lgkmcnt(1)\n"
      "v_exp_f32_e32 v140, v140\n"
      "v_accvgpr_read_b32 v237, a237\n"
      "v_mfma_f32_16x16x32_bf16 v[128:131], v[64:67], a[128:131], 0\n"
      "ds_read_b128 a[176:179], v244 offset:1024\n"
      "v_cvt_pk_bf16_f32 v164, v136, v137\n"
      "v_accvgpr_read_b32 v238, a238\n"
      "v_mfma_f32_16x16x32_bf16 v[128:131], v[68:71], a[132:135], v[128:131]\n"
      "v_exp_f32_e32 v141, v141\n"
      "v_accvgpr_read_b32 v239, a239\n"
      "v_mfma_f32_16x16x32_bf16 v[128:131], v[72:75], a[136:139], v[128:131]\n"
      "ds_read_b128 a[180:183], v244 offset:3072\n"
      "ds_read_b128 v[228:231], v247 offset:3072\n"
      "v_mfma_f32_16x16x32_bf16 a[240:243], a[120:123], a[120:123], a[240:243]\n"
      "v_cvt_pk_bf16_f32 v165, v138, v139\n"
      "v_mul_f32_e32 v236, v233, v236\n"
      "v_mfma_f32_16x16x32_bf16 v[128:131], v[76:79], a[140:143], v[128:131]\n"
      "v_exp_f32_e32 v142, v142\n"
      "v_mul_f32_e32 v237, v233, v237\n"
      "v_mfma_f32_16x16x32_bf16 v[132:135], v[92:95], a[140:143], 0\n"
      "ds_read_b128 a[184:187], v244 offset:5120\n"
      "v_cvt_pk_bf16_f32 v166, v140, v141\n"
      "v_mul_f32_e32 v238, v233, v238\n"
      "v_mfma_f32_16x16x32_bf16 v[132:135], v[88:91], a[136:139], v[132:135]\n"
      "v_exp_f32_e32 v143, v143\n"
      "v_mul_f32_e32 v239, v233, v239\n"
      "v_mfma_f32_16x16x32_bf16 v[132:135], v[84:87], a[132:135], v[132:135]\n"
      "ds_read_b128 a[188:191], v244 offset:7168\n"
      "v_cvt_pk_bf16_f32 v167, v142, v143\n"
      "v_cvt_pk_bf16_f32 v214, v236, v237\n"
      "v_mfma_f32_16x16x32_bf16 v[132:135], v[80:83], a[128:131], v[132:135]\n"
      "v_exp_f32_e32 v128, v128\n"
      "v_cvt_pk_bf16_f32 v215, v238, v239\n"
      "v_smfmac_f32_16x16x64_bf16 v[224:227], v[220:223], v[160:167], v241\n"
      "ds_write_b128 v242, v[164:167] offset:11264\n"
      "v_mfma_f32_16x16x32_bf16 v[176:179], a[64:67], a[160:163], v[176:179]\n"
      "buffer_store_dwordx4 v[212:215], v250, s[52:55], s59 offen offset:0 sc0 sc1\n"
      "v_mfma_f32_16x16x32_bf16 v[176:179], a[72:75], a[164:167], v[176:179]\n"
      "v_exp_f32_e32 v129, v129\n"
      "v_mfma_f32_16x16x32_bf16 v[176:179], a[80:83], a[168:171], v[176:179]\n"
      "s_waitcnt lgkmcnt(3)\n"
      "v_mfma_f32_16x16x32_bf16 v[176:179], a[88:91], a[172:175], v[176:179]\n"
      "v_exp_f32_e32 v130, v130\n"
      "v_add_f32_e32 v228, v228, v229\n"
      "v_mfma_f32_16x16x32_bf16 v[180:183], a[92:95], a[172:175], v[180:183]\n"
      "v_add_f32_e32 v230, v230, v231\n"
      "v_mfma_f32_16x16x32_bf16 v[180:183], a[84:87], a[168:171], v[180:183]\n"
      "v_exp_f32_e32 v131, v131\n"
      "v_add_f32_e32 v234, v228, v230\n"
      "v_mfma_f32_16x16x32_bf16 v[180:183], a[76:79], a[164:167], v[180:183]\n"
      "s_mov_b32 s59, 0x2000\n"
      "v_rcp_f32_e32 v232, v234\n"
      "v_mfma_f32_16x16x32_bf16 v[180:183], a[68:71], a[160:163], v[180:183]\n"
      "s_waitcnt lgkmcnt(1)\n"
      "v_exp_f32_e32 v132, v132\n"
      "v_mfma_f32_16x16x32_bf16 v[136:139], v[64:67], a[144:147], 0\n"
      "ds_read_b128 a[160:163], v245 offset:0\n"
      "v_cvt_pk_bf16_f32 v152, v128, v129\n"
      "v_mov_b32_e32 v233, v232\n"
      "v_mfma_f32_16x16x32_bf16 v[136:139], v[68:71], a[148:151], v[136:139]\n"
      "v_exp_f32_e32 v133, v133\n"
      "v_mfma_f32_16x16x32_bf16 v[136:139], v[72:75], a[152:155], v[136:139]\n"
      "ds_read_b128 a[164:167], v245 offset:2048\n"
      "v_cvt_pk_bf16_f32 v153, v130, v131\n"
      "v_med3_f32 v229, v234, 1.0, s63\n"
      "v_mfma_f32_16x16x32_bf16 v[136:139], v[76:79], a[156:159], v[136:139]\n"
      "v_exp_f32_e32 v134, v134\n"
      "v_cmp_neq_f32_e32 vcc, v229, v234\n"
      "v_mfma_f32_16x16x32_bf16 v[140:143], v[92:95], a[156:159], 0\n"
      "ds_read_b128 a[168:171], v245 offset:4096\n"
      "v_cvt_pk_bf16_f32 v154, v132, v133\n"
      "v_mfma_f32_16x16x32_bf16 v[140:143], v[88:91], a[152:155], v[140:143]\n"
      "v_exp_f32_e32 v135, v135\n"
      "v_mfma_f32_16x16x32_bf16 a[240:243], a[124:127], a[124:127], a[240:243]\n"
      "v_permlane32_swap_b32_e32 v232, v233\n"
      "v_mfma_f32_16x16x32_bf16 v[140:143], v[84:87], a[148:151], v[140:143]\n"
      "s_cmp_lg_u64 vcc, 0\n"
      "s_addc_u32 s66, s66, s66\n"
      "ds_read_b128 a[172:175], v245 offset:6144\n"
      "v_cvt_pk_bf16_f32 v155, v134, v135\n"
      "v_mfma_f32_16x16x32_bf16 v[140:143], v[80:83], a[144:147], v[140:143]\n"
      "v_exp_f32_e32 v136, v136\n"
      "v_mfma_f32_16x16x32_bf16 v[184:187], a[64:67], a[176:179], v[184:187]\n"
      "ds_write_b128 v242, v[152:155] offset:12288\n"
      "v_mfma_f32_16x16x32_bf16 v[184:187], a[72:75], a[180:183], v[184:187]\n"
      "v_exp_f32_e32 v137, v137\n"
      "v_mul_f32_e32 v192, v232, v192\n"
      "v_mfma_f32_16x16x32_bf16 v[184:187], a[80:83], a[184:187], v[184:187]\n"
      "v_mul_f32_e32 v193, v232, v193\n"
      "v_mfma_f32_16x16x32_bf16 v[184:187], a[88:91], a[188:191], v[184:187]\n"
      "v_exp_f32_e32 v138, v138\n"
      "v_mul_f32_e32 v194, v232, v194\n"
      "v_mfma_f32_16x16x32_bf16 v[188:191], a[92:95], a[188:191], v[188:191]\n"
      "v_mul_f32_e32 v195, v232, v195\n"
      "v_mfma_f32_16x16x32_bf16 v[188:191], a[84:87], a[184:187], v[188:191]\n"
      "v_exp_f32_e32 v139, v139\n"
      "v_cvt_pk_bf16_f32 v208, v192, v193\n"
      "v_mfma_f32_16x16x32_bf16 v[188:191], a[76:79], a[180:183], v[188:191]\n"
      "v_cvt_pk_bf16_f32 v209, v194, v195\n"
      "v_mfma_f32_16x16x32_bf16 v[188:191], a[68:71], a[176:179], v[188:191]\n"
      "s_waitcnt lgkmcnt(1)\n"
      "v_exp_f32_e32 v140, v140\n"
      "v_mfma_f32_16x16x32_bf16 v[128:131], v[96:99], a[128:131], 0\n"
      "ds_read_b128 a[176:179], v245 offset:1024\n"
      "v_cvt_pk_bf16_f32 v168, v136, v137\n"
      "v_mul_f32_e32 v196, v232, v196\n"
      "v_mfma_f32_16x16x32_bf16 v[128:131], v[100:103], a[132:135], v[128:131]\n"
      "v_exp_f32_e32 v141, v141\n"
      "v_mfma_f32_16x16x32_bf16 v[128:131], v[104:107], a[136:139], v[128:131]\n"
      "ds_read_b128 a[180:183], v245 offset:3072\n"
      "v_cvt_pk_bf16_f32 v169, v138, v139\n"
      "v_mul_f32_e32 v197, v232, v197\n"
      "v_mfma_f32_16x16x32_bf16 v[128:131], v[108:111], a[140:143], v[128:131]\n"
      "v_exp_f32_e32 v142, v142\n"
      "v_mfma_f32_16x16x32_bf16 v[132:135], v[124:127], a[140:143], 0\n"
      "ds_read_b128 a[184:187], v245 offset:5120\n"
      "v_cvt_pk_bf16_f32 v170, v140, v141\n"
      "v_mul_f32_e32 v198, v232, v198\n"
      "v_mfma_f32_16x16x32_bf16 v[132:135], v[120:123], a[136:139], v[132:135]\n"
      "v_exp_f32_e32 v143, v143\n"
      "v_mfma_f32_16x16x32_bf16 v[132:135], v[116:119], a[132:135], v[132:135]\n"
      "ds_read_b128 a[188:191], v245 offset:7168\n"
      "v_cvt_pk_bf16_f32 v171, v142, v143\n"
      "v_mfma_f32_16x16x32_bf16 v[132:135], v[112:115], a[128:131], v[132:135]\n"
      "v_exp_f32_e32 v128, v128\n"
      "v_mul_f32_e32 v199, v232, v199\n"
      "v_mfma_f32_16x16x32_bf16 v[176:179], a[96:99], a[160:163], v[176:179]\n"
      "ds_write_b128 v242, v[168:171] offset:13312\n"
      "v_mfma_f32_16x16x32_bf16 v[176:179], a[104:107], a[164:167], v[176:179]\n"
      "v_exp_f32_e32 v129, v129\n"
      "v_mfma_f32_16x16x32_bf16 v[176:179], a[112:115], a[168:171], v[176:179]\n"
      "v_cvt_pk_bf16_f32 v210, v196, v197\n"
      "v_cvt_pk_bf16_f32 v211, v198, v199\n"
      "v_mfma_f32_16x16x32_bf16 v[176:179], a[120:123], a[172:175], v[176:179]\n"
      "v_exp_f32_e32 v130, v130\n"
      "v_mfma_f32_16x16x32_bf16 v[180:183], a[124:127], a[172:175], v[180:183]\n"
      "buffer_store_dwordx4 v[208:211], v249, s[52:55], s59 offen offset:0 sc0 sc1\n"
      "v_mfma_f32_16x16x32_bf16 v[180:183], a[116:119], a[168:171], v[180:183]\n"
      "v_exp_f32_e32 v131, v131\n"
      "v_mul_f32_e32 v200, v233, v200\n"
      "v_mfma_f32_16x16x32_bf16 v[180:183], a[108:111], a[164:167], v[180:183]\n"
      "v_mul_f32_e32 v201, v233, v201\n"
      "v_mfma_f32_16x16x32_bf16 v[180:183], a[100:103], a[160:163], v[180:183]\n"
      "s_waitcnt lgkmcnt(1)\n"
      "v_exp_f32_e32 v132, v132\n"
      "v_mul_f32_e32 v202, v233, v202\n"
      "v_mfma_f32_16x16x32_bf16 v[136:139], v[96:99], a[144:147], 0\n"
      "ds_read_b128 a[128:131], v240 offset:0\n"
      "v_cvt_pk_bf16_f32 v156, v128, v129\n"
      "v_mfma_f32_16x16x32_bf16 v[136:139], v[100:103], a[148:151], v[136:139]\n"
      "v_exp_f32_e32 v133, v133\n"
      "v_mul_f32_e32 v203, v233, v203\n"
      "v_mfma_f32_16x16x32_bf16 v[136:139], v[104:107], a[152:155], v[136:139]\n"
      "ds_read_b128 a[132:135], v240 offset:256\n"
      "v_cvt_pk_bf16_f32 v157, v130, v131\n"
      "v_mfma_f32_16x16x32_bf16 v[136:139], v[108:111], a[156:159], v[136:139]\n"
      "v_exp_f32_e32 v134, v134\n"
      "v_cvt_pk_bf16_f32 v212, v200, v201\n"
      "v_mfma_f32_16x16x32_bf16 v[140:143], v[124:127], a[156:159], 0\n"
      "ds_read_b128 a[136:139], v240 offset:512\n"
      "v_cvt_pk_bf16_f32 v158, v132, v133\n"
      "v_mfma_f32_16x16x32_bf16 v[140:143], v[120:123], a[152:155], v[140:143]\n"
      "v_exp_f32_e32 v135, v135\n"
      "v_mfma_f32_16x16x32_bf16 v[140:143], v[116:119], a[148:151], v[140:143]\n"
      "ds_read_b128 a[140:143], v240 offset:768\n"
      "v_cvt_pk_bf16_f32 v159, v134, v135\n"
      "v_cvt_pk_bf16_f32 v213, v202, v203\n"
      "v_mfma_f32_16x16x32_bf16 v[140:143], v[112:115], a[144:147], v[140:143]\n"
      "v_exp_f32_e32 v136, v136\n"
      "v_smfmac_f32_16x16x64_bf16 v[224:227], v[216:219], v[152:159], v241\n"
      "ds_write_b128 v242, v[156:159] offset:14336\n"
      "v_mfma_f32_16x16x32_bf16 v[184:187], a[96:99], a[176:179], v[184:187]\n"
      "v_mul_f32_e32 v204, v233, v204\n"
      "v_mul_f32_e32 v205, v233, v205\n"
      "v_mfma_f32_16x16x32_bf16 v[184:187], a[104:107], a[180:183], v[184:187]\n"
      "v_exp_f32_e32 v137, v137\n"
      "v_mfma_f32_16x16x32_bf16 v[184:187], a[112:115], a[184:187], v[184:187]\n"
      "v_mul_f32_e32 v206, v233, v206\n"
      "v_mfma_f32_16x16x32_bf16 v[184:187], a[120:123], a[188:191], v[184:187]\n"
      "v_exp_f32_e32 v138, v138\n"
      "v_mul_f32_e32 v207, v233, v207\n"
      "v_mfma_f32_16x16x32_bf16 v[188:191], a[124:127], a[188:191], v[188:191]\n"
      "v_cvt_pk_bf16_f32 v214, v204, v205\n"
      "v_mfma_f32_16x16x32_bf16 v[188:191], a[116:119], a[184:187], v[188:191]\n"
      "v_exp_f32_e32 v139, v139\n"
      "v_cvt_pk_bf16_f32 v215, v206, v207\n"
      "v_mfma_f32_16x16x32_bf16 v[188:191], a[108:111], a[180:183], v[188:191]\n"
      "buffer_store_dwordx4 v[212:215], v250, s[52:55], s59 offen offset:0 sc0 sc1\n"
      "v_mfma_f32_16x16x32_bf16 v[188:191], a[100:103], a[176:179], v[188:191]\n"
      "s_waitcnt lgkmcnt(1)\n"
      "v_exp_f32_e32 v140, v140\n"
      "v_accvgpr_read_b32 v236, a240\n"
      "v_accvgpr_read_b32 v237, a241\n"
      "v_accvgpr_read_b32 v238, a242\n"
      "v_accvgpr_read_b32 v239, a243\n"
      "v_add_f32_e32 v236, v236, v237\n"
      "v_add_f32_e32 v238, v238, v239\n"
      "v_add_f32_e32 v236, v236, v238\n"
      "v_mov_b32_e32 v237, 0x207\n"
      "v_cmp_class_f32_e32 vcc, v236, v237\n"
      "s_nop 3\n"
      "s_cmp_lg_u64 vcc, 0\n"
      "s_cselect_b32 s28, 0x7fc00000, 0\n"
      "s_and_b32 s68, s56, 1\n"
      "s_cmp_eq_u32 s68, 1\n"
      "s_cbranch_scc1 .LloopB1_%=\n"
      "s_branch .LloopB0_%=\n"
      ".p2align 6\n"
      ".Lloop0_%=:\n"
      "v_mfma_f32_16x16x32_bf16 v[128:131], v[0:3], a[128:131], 0\n"
      "ds_read_b128 a[144:147], v240 offset:12288\n"
      "v_cvt_pk_bf16_f32 v172, v136, v137\n"
      "v_mfma_f32_16x16x32_bf16 v[128:131], v[4:7], a[132:135], v[128:131]\n"
      "v_exp_f32_e32 v141, v141\n"
      "v_mfma_f32_16x16x32_bf16 v[128:131], v[8:11], a[136:139], v[128:131]\n"
      "ds_read_b128 a[148:151], v240 offset:12544\n"
      "v_cvt_pk_bf16_f32 v173, v138, v139\n"
      "v_mfma_f32_16x16x32_bf16 v[128:131], v[12:15], a[140:143], v[128:131]\n"
      "v_exp_f32_e32 v142, v142\n"
      "v_mfma_f32_16x16x32_bf16 v[132:135], v[28:31], a[140:143], 0\n"
      "ds_read_b128 a[152:155], v240 offset:12800\n"
      "v_cvt_pk_bf16_f32 v174, v140, v141\n"
      "v_mfma_f32_16x16x32_bf16 v[132:135], v[24:27], a[136:139], v[132:135]\n"
      "v_exp_f32_e32 v143, v143\n"
      "v_mfma_f32_16x16x32_bf16 v[132:135], v[20:23], a[132:135], v[132:135]\n"
      "ds_read_b128 a[156:159], v240 offset:13056\n"
      "v_cvt_pk_bf16_f32 v175, v142, v143\n"
      "v_mfma_f32_16x16x32_bf16 v[132:135], v[16:19], a[128:131], v[132:135]\n"
      "v_exp_f32_e32 v128, v128\n"
      "v_smfmac_f32_16x16x64_bf16 v[224:227], v[220:223], v[168:175], v241\n"
      "ds_write_b128 v242, v[172:175] offset:7168\n"
      "v_mfma_f32_16x16x32_bf16 v[176:179], a[0:3], v[144:147], 0\n"
      "v_xor_b32_e32 v240, 0x4000, v240\n"
      "v_mfma_f32_16x16x32_bf16 v[176:179], a[8:11], v[148:151], v[176:179]\n"
      "v_exp_f32_e32 v129, v129\n"
      "v_mfma_f32_16x16x32_bf16 v[176:179], a[16:19], v[152:155], v[176:179]\n"
      "v_mfma_f32_16x16x32_bf16 v[176:179], a[24:27], v[156:159], v[176:179]\n"
      "v_exp_f32_e32 v130, v130\n"
      "v_mfma_f32_16x16x32_bf16 v[180:183], a[28:31], v[156:159], 0\n"
      "s_waitcnt vmcnt(10) lgkmcnt(0)\n"
      "s_barrier\n"
      "v_mfma_f32_16x16x32_bf16 v[180:183], a[20:23], v[152:155], v[180:183]\n"
      "v_exp_f32_e32 v131, v131\n"
      "v_mfma_f32_16x16x32_bf16 v[180:183], a[12:15], v[148:151], v[180:183]\n"
      "v_add_f32_e32 v235, v224, v225\n"
      "v_mov_b32_e32 v224, s28\n"
      "v_mfma_f32_16x16x32_bf16 v[180:183], a[4:7], v[144:147], v[180:183]\n"
      "v_exp_f32_e32 v132, v132\n"
      "v_mfma_f32_16x16x32_bf16 v[136:139], v[0:3], a[144:147], 0\n"
      "ds_read_b128 a[160:163], v243 offset:0\n"
      "v_cvt_pk_bf16_f32 v144, v128, v129\n"
      "v_mfma_f32_16x16x32_bf16 v[136:139], v[4:7], a[148:151], v[136:139]\n"
      "s_add_u32 s58, s58, 0x2000\n"
      "v_exp_f32_e32 v133, v133\n"
      "v_mfma_f32_16x16x32_bf16 v[136:139], v[8:11], a[152:155], v[136:139]\n"
      "ds_read_b128 a[164:167], v243 offset:2048\n"
      "v_cvt_pk_bf16_f32 v145, v130, v131\n"
      "v_mfma_f32_16x16x32_bf16 v[136:139], v[12:15], a[156:159], v[136:139]\n"
      "s_min_u32 s58, s58, s67\n"
      "v_exp_f32_e32 v134, v134\n"
      "v_mfma_f32_16x16x32_bf16 v[140:143], v[28:31], a[156:159], 0\n"
      "ds_read_b128 a[168:171], v243 offset:4096\n"
      "v_cvt_pk_bf16_f32 v146, v132, v133\n"
      "v_mfma_f32_16x16x32_bf16 v[140:143], v[24:27], a[152:155], v[140:143]\n"
      "s_xor_b32 m0, m0, 0x2000\n"
      "v_exp_f32_e32 v135, v135\n"
      "v_mfma_f32_16x16x32_bf16 v[140:143], v[20:23], a[148:151], v[140:143]\n"
      "ds_read_b128 a[172:175], v243 offset:6144\n"
      "v_cvt_pk_bf16_f32 v147, v134, v135\n"
      "v_mfma_f32_16x16x32_bf16 v[140:143], v[16:19], a[144:147], v[140:143]\n"
      "v_exp_f32_e32 v136, v136\n"
      "v_mfma_f32_16x16x32_bf16 v[184:187], a[0:3], v[160:163], 0\n"
      "ds_write_b128 v242, v[144:147] offset:8192\n"
      "v_mfma_f32_16x16x32_bf16 v[184:187], a[8:11], v[164:167], v[184:187]\n"
      "v_exp_f32_e32 v137, v137\n"
      "v_mfma_f32_16x16x32_bf16 v[184:187], a[16:19], v[168:171], v[184:187]\n"
      "buffer_load_dwordx4 v248, s[40:43], s58 offen offset:0 lds\n"
      "v_mov_b32_e32 v225, 0\n"
      "v_mfma_f32_16x16x32_bf16 v[184:187], a[24:27], v[172:175], v[184:187]\n"
      "v_exp_f32_e32 v138, v138\n"
      "v_mfma_f32_16x16x32_bf16 v[188:191], a[28:31], v[172:175], 0\n"
      "ds_read_b128 v[228:231], v247 offset:1024\n"
      "v_mfma_f32_16x16x32_bf16 v[188:191], a[20:23], v[168:171], v[188:191]\n"
      "v_exp_f32_e32 v139, v139\n"
      "v_mfma_f32_16x16x32_bf16 v[188:191], a[12:15], v[164:167], v[188:191]\n"
      "ds_write_b32 v246, v235 offset:0\n"
      "v_mfma_f32_16x16x32_bf16 v[188:191], a[4:7], v[160:163], v[188:191]\n"
      "s_waitcnt lgkmcnt(3)\n"
      "v_exp_f32_e32 v140, v140\n"
      "v_mfma_f32_16x16x32_bf16 v[128:131], v[32:35], a[128:131], 0\n"
      "ds_read_b128 a[176:179], v243 offset:1024\n"
      "v_cvt_pk_bf16_f32 v160, v136, v137\n"
      "v_mfma_f32_16x16x32_bf16 v[128:131], v[36:39], a[132:135], v[128:131]\n"
      "v_exp_f32_e32 v141, v141\n"
      "v_mfma_f32_16x16x32_bf16 v[128:131], v[40:43], a[136:139], v[128:131]\n"
      "ds_read_b128 a[180:183], v243 offset:3072\n"
      "v_cvt_pk_bf16_f32 v161, v138, v139\n"
      "v_mfma_f32_16x16x32_bf16 v[128:131], v[44:47], a[140:143], v[128:131]\n"
      "v_exp_f32_e32 v142, v142\n"
      "v_mfma_f32_16x16x32_bf16 v[132:135], v[60:63], a[140:143], 0\n"
      "ds_read_b128 a[184:187], v243 offset:5120\n"
      "v_cvt_pk_bf16_f32 v162, v140, v141\n"
      "v_mfma_f32_16x16x32_bf16 v[132:135], v[56:59], a[136:139], v[132:135]\n"
      "v_exp_f32_e32 v143, v143\n"
      "v_mfma_f32_16x16x32_bf16 v[132:135], v[52:55], a[132:135], v[132:135]\n"
      "ds_read_b128 a[188:191], v243 offset:7168\n"
      "v_cvt_pk_bf16_f32 v163, v142, v143\n"
      "v_mfma_f32_16x16x32_bf16 v[132:135], v[48:51], a[128:131], v[132:135]\n"
      "s_waitcnt lgkmcnt(5)\n"
      "v_exp_f32_e32 v128, v128\n"
      "v_mfma_f32_16x16x32_bf16 v[176:179], a[32:35], a[160:163], v[176:179]\n"
      "ds_write_b128 v242, v[160:163] offset:9216\n"
      "v_mfma_f32_16x16x32_bf16 v[176:179], a[40:43], a[164:167], v[176:179]\n"
      "v_exp_f32_e32 v129, v129\n"
      "v_mfma_f32_16x16x32_bf16 v[176:179], a[48:51], a[168:171], v[176:179]\n"
      "buffer_load_dwordx4 v248, s[40:43], s58 offen offset:1024 lds\n"
      "v_add_f32_e32 v228, v228, v229\n"
      "v_mfma_f32_16x16x32_bf16 v[176:179], a[56:59], a[172:175], v[176:179]\n"
      "v_exp_f32_e32 v130, v130\n"
      "v_mfma_f32_16x16x32_bf16 v[180:183], a[60:63], a[172:175], v[180:183]\n"
      "v_add_f32_e32 v230, v230, v231\n"
      "v_add_f32_e32 v234, v228, v230\n"
      "v_mfma_f32_16x16x32_bf16 v[180:183], a[52:55], a[168:171], v[180:183]\n"
      "v_exp_f32_e32 v131, v131\n"
      "v_mfma_f32_16x16x32_bf16 v[180:183], a[44:47], a[164:167], v[180:183]\n"
      "v_rcp_f32_e32 v232, v234\n"
      "v_mfma_f32_16x16x32_bf16 v[180:183], a[36:39], a[160:163], v[180:183]\n"
      "s_waitcnt lgkmcnt(1)\n"
      "v_exp_f32_e32 v132, v132\n"
      "v_mfma_f32_16x16x32_bf16 v[136:139], v[32:35], a[144:147], 0\n"
      "ds_read_b128 a[160:163], v244 offset:0\n"
      "v_cvt_pk_bf16_f32 v148, v128, v129\n"
      "v_mfma_f32_16x16x32_bf16 v[136:139], v[36:39], a[148:151], v[136:139]\n"
      "v_exp_f32_e32 v133, v133\n"
      "v_mfma_f32_16x16x32_bf16 v[136:139], v[40:43], a[152:155], v[136:139]\n"
      "ds_read_b128 a[164:167], v244 offset:2048\n"
      "v_cvt_pk_bf16_f32 v149, v130, v131\n"
      "v_mfma_f32_16x16x32_bf16 v[136:139], v[44:47], a[156:159], v[136:139]\n"
      "v_exp_f32_e32 v134, v134\n"
      "v_mfma_f32_16x16x32_bf16 v[140:143], v[60:63], a[156:159], 0\n"
      "ds_read_b128 a[168:171], v244 offset:4096\n"
      "v_cvt_pk_bf16_f32 v150, v132, v133\n"
      "v_mfma_f32_16x16x32_bf16 v[140:143], v[56:59], a[152:155], v[140:143]\n"
      "v_exp_f32_e32 v135, v135\n"
      "v_mfma_f32_16x16x32_bf16 v[140:143], v[52:55], a[148:151], v[140:143]\n"
      "ds_read_b128 a[172:175], v244 offset:6144\n"
      "v_cvt_pk_bf16_f32 v151, v134, v135\n"
      "v_mfma_f32_16x16x32_bf16 v[140:143], v[48:51], a[144:147], v[140:143]\n"
      "v_exp_f32_e32 v136, v136\n"
      "v_smfmac_f32_16x16x64_bf16 v[224:227], v[216:219], v[144:151], v241\n"
      "ds_write_b128 v242, v[148:151] offset:10240\n"
      "v_mfma_f32_16x16x32_bf16 v[184:187], a[32:35], a[176:179], v[184:187]\n"
      "s_add_u32 s59, s59, 0x2000\n"
      "v_mov_b32_e32 v233, v232\n"
      "v_med3_f32 v229, v234, 1.0, s63\n"
      "v_mfma_f32_16x16x32_bf16 v[184:187], a[40:43], a[180:183], v[184:187]\n"
      "v_exp_f32_e32 v137, v137\n"
      "v_mfma_f32_16x16x32_bf16 v[184:187], a[48:51], a[184:187], v[184:187]\n"
      "v_permlane32_swap_b32_e32 v232, v233\n"
      "v_mfma_f32_16x16x32_bf16 v[184:187], a[56:59], a[188:191], v[184:187]\n"
      "v_exp_f32_e32 v138, v138\n"
      "v_mfma_f32_16x16x32_bf16 v[188:191], a[60:63], a[188:191], v[188:191]\n"
      "v_cmp_neq_f32_e32 vcc, v229, v234\n"
      "v_mul_f32_e32 v192, v232, v192\n"
      "v_mfma_f32_16x16x32_bf16 v[188:191], a[52:55], a[184:187], v[188:191]\n"
      "v_exp_f32_e32 v139, v139\n"
      "v_mfma_f32_16x16x32_bf16 v[188:191], a[44:47], a[180:183], v[188:191]\n"
      "v_mul_f32_e32 v193, v232, v193\n"
      "v_mul_f32_e32 v194, v232, v194\n"
      "v_mfma_f32_16x16x32_bf16 v[188:191], a[36:39], a[176:179], v[188:191]\n"
      "s_waitcnt lgkmcnt(1)\n"
      "v_exp_f32_e32 v140, v140\n"
      "v_mfma_f32_16x16x32_bf16 v[128:131], v[64:67], a[128:131], 0\n"
      "ds_read_b128 a[176:179], v244 offset:1024\n"
      "v_cvt_pk_bf16_f32 v164, v136, v137\n"
      "v_mfma_f32_16x16x32_bf16 v[128:131], v[68:71], a[132:135], v[128:131]\n"
      "s_cmp_lg_u64 vcc, 0\n"
      "v_exp_f32_e32 v141, v141\n"
      "v_mfma_f32_16x16x32_bf16 v[128:131], v[72:75], a[136:139], v[128:131]\n"
      "ds_read_b128 a[180:183], v244 offset:3072\n"
      "v_cvt_pk_bf16_f32 v165, v138, v139\n"
      "v_mfma_f32_16x16x32_bf16 v[128:131], v[76:79], a[140:143], v[128:131]\n"
      "s_addc_u32 s66, s66, s66\n"
      "v_exp_f32_e32 v142, v142\n"
      "v_mfma_f32_16x16x32_bf16 v[132:135], v[92:95], a[140:143], 0\n"
      "ds_read_b128 a[184:187], v244 offset:5120\n"
      "v_cvt_pk_bf16_f32 v166, v140, v141\n"
      "v_mfma_f32_16x16x32_bf16 v[132:135], v[88:91], a[136:139], v[132:135]\n"
      "v_exp_f32_e32 v143, v143\n"
      "v_mfma_f32_16x16x32_bf16 v[132:135], v[84:87], a[132:135], v[132:135]\n"
      "ds_read_b128 a[188:191], v244 offset:7168\n"
      "v_cvt_pk_bf16_f32 v167, v142, v143\n"
      "v_mfma_f32_16x16x32_bf16 v[132:135], v[80:83], a[128:131], v[132:135]\n"
      "v_exp_f32_e32 v128, v128\n"
      "v_smfmac_f32_16x16x64_bf16 v[224:227], v[220:223], v[160:167], v241\n"
      "ds_write_b128 v242, v[164:167] offset:11264\n"
      "v_mfma_f32_16x16x32_bf16 v[176:179], a[64:67], a[160:163], v[176:179]\n"
      "v_mul_f32_e32 v195, v232, v195\n"
      "v_cvt_pk_bf16_f32 v208, v192, v193\n"
      "v_mfma_f32_16x16x32_bf16 v[176:179], a[72:75], a[164:167], v[176:179]\n"
      "v_exp_f32_e32 v129, v129\n"
      "v_mfma_f32_16x16x32_bf16 v[176:179], a[80:83], a[168:171], v[176:179]\n"
      "v_cvt_pk_bf16_f32 v209, v194, v195\n"
      "v_mfma_f32_16x16x32_bf16 v[176:179], a[88:91], a[172:175], v[176:179]\n"
      "v_exp_f32_e32 v130, v130\n"
      "v_mfma_f32_16x16x32_bf16 v[180:183], a[92:95], a[172:175], v[180:183]\n"
      "v_mul_f32_e32 v196, v232, v196\n"
      "v_mul_f32_e32 v197, v232, v197\n"
      "v_mfma_f32_16x16x32_bf16 v[180:183], a[84:87], a[168:171], v[180:183]\n"
      "v_exp_f32_e32 v131, v131\n"
      "v_mfma_f32_16x16x32_bf16 v[180:183], a[76:79], a[164:167], v[180:183]\n"
      "v_mul_f32_e32 v198, v232, v198\n"
      "v_mul_f32_e32 v199, v232, v199\n"
      "v_mfma_f32_16x16x32_bf16 v[180:183], a[68:71], a[160:163], v[180:183]\n"
      "s_waitcnt lgkmcnt(1)\n"
      "v_exp_f32_e32 v132, v132\n"
      "v_mfma_f32_16x16x32_bf16 v[136:139], v[64:67], a[144:147], 0\n"
      "ds_read_b128 a[160:163], v245 offset:0\n"
      "v_cvt_pk_bf16_f32 v152, v128, v129\n"
      "v_mfma_f32_16x16x32_bf16 v[136:139], v[68:71], a[148:151], v[136:139]\n"
      "v_exp_f32_e32 v133, v133\n"
      "v_mfma_f32_16x16x32_bf16 v[136:139], v[72:75], a[152:155], v[136:139]\n"
      "ds_read_b128 a[164:167], v245 offset:2048\n"
      "v_cvt_pk_bf16_f32 v153, v130, v131\n"
      "v_mfma_f32_16x16x32_bf16 v[136:139], v[76:79], a[156:159], v[136:139]\n"
      "v_exp_f32_e32 v134, v134\n"
      "v_mfma_f32_16x16x32_bf16 v[140:143], v[92:95], a[156:159], 0\n"
      "ds_read_b128 a[168:171], v245 offset:4096\n"
      "v_cvt_pk_bf16_f32 v154, v132, v133\n"
      "v_mfma_f32_16x16x32_bf16 v[140:143], v[88:91], a[152:155], v[140:143]\n"
      "v_exp_f32_e32 v135, v135\n"
      "v_mfma_f32_16x16x32_bf16 v[140:143], v[84:87], a[148:151], v[140:143]\n"
      "ds_read_b128 a[172:175], v245 offset:6144\n"
      "v_cvt_pk_bf16_f32 v155, v134, v135\n"
      "v_mfma_f32_16x16x32_bf16 v[140:143], v[80:83], a[144:147], v[140:143]\n"
      "v_exp_f32_e32 v136, v136\n"
      "v_mfma_f32_16x16x32_bf16 v[184:187], a[64:67], a[176:179], v[184:187]\n"
      "ds_write_b128 v242, v[152:155] offset:12288\n"
      "v_mfma_f32_16x16x32_bf16 v[184:187], a[72:75], a[180:183], v[184:187]\n"
      "v_exp_f32_e32 v137, v137\n"
      "v_mfma_f32_16x16x32_bf16 v[184:187], a[80:83], a[184:187], v[184:187]\n"
      "v_cvt_pk_bf16_f32 v210, v196, v197\n"
      "v_mfma_f32_16x16x32_bf16 v[184:187], a[88:91], a[188:191], v[184:187]\n"
      "v_exp_f32_e32 v138, v138\n"
      "v_mfma_f32_16x16x32_bf16 v[188:191], a[92:95], a[188:191], v[188:191]\n"
      "v_cvt_pk_bf16_f32 v211, v198, v199\n"
      "v_mfma_f32_16x16x32_bf16 v[188:191], a[84:87], a[184:187], v[188:191]\n"
      "v_exp_f32_e32 v139, v139\n"
      "v_mfma_f32_16x16x32_bf16 v[188:191], a[76:79], a[180:183], v[188:191]\n"
      "buffer_store_dwordx4 v[208:211], v249, s[52:55], s59 offen offset:0 sc0 sc1\n"
      "v_mfma_f32_16x16x32_bf16 v[188:191], a[68:71], a[176:179], v[188:191]\n"
      "s_waitcnt lgkmcnt(1)\n"
      "v_exp_f32_e32 v140, v140\n"
      "v_mfma_f32_16x16x32_bf16 v[128:131], v[96:99], a[128:131], 0\n"
      "ds_read_b128 a[176:179], v245 offset:1024\n"
      "v_cvt_pk_bf16_f32 v168, v136, v137\n"
      "v_mfma_f32_16x16x32_bf16 v[128:131], v[100:103], a[132:135], v[128:131]\n"
      "v_exp_f32_e32 v141, v141\n"
      "v_mfma_f32_16x16x32_bf16 v[128:131], v[104:107], a[136:139], v[128:131]\n"
      "ds_read_b128 a[180:183], v245 offset:3072\n"
      "v_cvt_pk_bf16_f32 v169, v138, v139\n"
      "v_mfma_f32_16x16x32_bf16 v[128:131], v[108:111], a[140:143], v[128:131]\n"
      "v_exp_f32_e32 v142, v142\n"
      "v_mfma_f32_16x16x32_bf16 v[132:135], v[124:127], a[140:143], 0\n"
      "ds_read_b128 a[184:187], v245 offset:5120\n"
      "v_cvt_pk_bf16_f32 v170, v140, v141\n"
      "v_mfma_f32_16x16x32_bf16 v[132:135], v[120:123], a[136:139], v[132:135]\n"
      "v_exp_f32_e32 v143, v143\n"
      "v_mfma_f32_16x16x32_bf16 v[132:135], v[116:119], a[132:135], v[132:135]\n"
      "ds_read_b128 a[188:191], v245 offset:7168\n"
      "v_cvt_pk_bf16_f32 v171, v142, v143\n"
      "v_mfma_f32_16x16x32_bf16 v[132:135], v[112:115], a[128:131], v[132:135]\n"
      "v_exp_f32_e32 v128, v128\n"
      "v_mfma_f32_16x16x32_bf16 v[176:179], a[96:99], a[160:163], v[176:179]\n"
      "ds_write_b128 v242, v[168:171] offset:13312\n"
      "v_mfma_f32_16x16x32_bf16 v[176:179], a[104:107], a[164:167], v[176:179]\n"
      "v_exp_f32_e32 v129, v129\n"
      "v_mfma_f32_16x16x32_bf16 v[176:179], a[112:115], a[168:171], v[176:179]\n"
      "v_mul_f32_e32 v200, v233, v200\n"
      "v_mul_f32_e32 v201, v233, v201\n"
      "v_mfma_f32_16x16x32_bf16 v[176:179], a[120:123], a[172:175], v[176:179]\n"
      "v_exp_f32_e32 v130, v130\n"
      "v_mfma_f32_16x16x32_bf16 v[180:183], a[124:127], a[172:175], v[180:183]\n"
      "v_mul_f32_e32 v202, v233, v202\n"
      "v_mul_f32_e32 v203, v233, v203\n"
      "v_mfma_f32_16x16x32_bf16 v[180:183], a[116:119], a[168:171], v[180:183]\n"
      "v_exp_f32_e32 v131, v131\n"
      "v_mfma_f32_16x16x32_bf16 v[180:183], a[108:111], a[164:167], v[180:183]\n"
      "v_cvt_pk_bf16_f32 v212, v200, v201\n"
      "v_cvt_pk_bf16_f32 v213, v202, v203\n"
      "v_mfma_f32_16x16x32_bf16 v[180:183], a[100:103], a[160:163], v[180:183]\n"
      "s_waitcnt lgkmcnt(1)\n"
      "v_exp_f32_e32 v132, v132\n"
      "v_mfma_f32_16x16x32_bf16 v[136:139], v[96:99], a[144:147], 0\n"
      "ds_read_b128 a[128:131], v240 offset:0\n"
      "v_cvt_pk_bf16_f32 v156, v128, v129\n"
      "v_mfma_f32_16x16x32_bf16 v[136:139], v[100:103], a[148:151], v[136:139]\n"
      "v_exp_f32_e32 v133, v133\n"
      "v_mfma_f32_16x16x32_bf16 v[136:139], v[104:107], a[152:155], v[136:139]\n"
      "ds_read_b128 a[132:135], v240 offset:256\n"
      "v_cvt_pk_bf16_f32 v157, v130, v131\n"
      "v_mfma_f32_16x16x32_bf16 v[136:139], v[108:111], a[156:159], v[136:139]\n"
      "v_exp_f32_e32 v134, v134\n"
      "v_mfma_f32_16x16x32_bf16 v[140:143], v[124:127], a[156:159], 0\n"
      "ds_read_b128 a[136:139], v240 offset:512\n"
      "v_cvt_pk_bf16_f32 v158, v132, v133\n"
      "v_mfma_f32_16x16x32_bf16 v[140:143], v[120:123], a[152:155], v[140:143]\n"
      "v_exp_f32_e32 v135, v135\n"
      "v_mfma_f32_16x16x32_bf16 v[140:143], v[116:119], a[148:151], v[140:143]\n"
      "ds_read_b128 a[140:143], v240 offset:768\n"
      "v_cvt_pk_bf16_f32 v159, v134, v135\n"
      "v_mfma_f32_16x16x32_bf16 v[140:143], v[112:115], a[144:147], v[140:143]\n"
      "v_exp_f32_e32 v136, v136\n"
      "v_smfmac_f32_16x16x64_bf16 v[224:227], v[216:219], v[152:159], v241\n"
      "ds_write_b128 v242, v[156:159] offset:14336\n"
      "v_mfma_f32_16x16x32_bf16 v[184:187], a[96:99], a[176:179], v[184:187]\n"
      "v_mul_f32_e32 v204, v233, v204\n"
      "v_mul_f32_e32 v205, v233, v205\n"
      "v_mfma_f32_16x16x32_bf16 v[184:187], a[104:107], a[180:183], v[184:187]\n"
      "v_exp_f32_e32 v137, v137\n"
      "v_mfma_f32_16x16x32_bf16 v[184:187], a[112:115], a[184:187], v[184:187]\n"
      "v_mul_f32_e32 v206, v233, v206\n"
      "v_mul_f32_e32 v207, v233, v207\n"
      "v_mfma_f32_16x16x32_bf16 v[184:187], a[120:123], a[188:191], v[184:187]\n"
      "v_exp_f32_e32 v138, v138\n"
      "v_mfma_f32_16x16x32_bf16 v[188:191], a[124:127], a[188:191], v[188:191]\n"
      "v_cvt_pk_bf16_f32 v214, v204, v205\n"
      "v_cvt_pk_bf16_f32 v215, v206, v207\n"
      "v_mfma_f32_16x16x32_bf16 v[188:191], a[116:119], a[184:187], v[188:191]\n"
      "v_exp_f32_e32 v139, v139\n"
      "v_mfma_f32_16x16x32_bf16 v[188:191], a[108:111], a[180:183], v[188:191]\n"
      "buffer_store_dwordx4 v[212:215], v250, s[52:55], s59 offen offset:0 sc0 sc1\n"
      "v_mfma_f32_16x16x32_bf16 v[188:191], a[100:103], a[176:179], v[188:191]\n"
      ".Lst3_%=:\n"
      "s_cbranch_execz .Lstback_%=\n"
      "s_waitcnt lgkmcnt(1)\n"
      "v_exp_f32_e32 v140, v140\n"
      ".LloopB0_%=:\n"
      "v_mfma_f32_16x16x32_bf16 v[128:131], v[0:3], a[128:131], 0\n"
      "ds_read_b128 a[144:147], v240 offset:4096\n"
      "v_cvt_pk_bf16_f32 v172, v136, v137\n"
      "v_mfma_f32_16x16x32_bf16 v[128:131], v[4:7], a[132:135], v[128:131]\n"
      "v_exp_f32_e32 v141, v141\n"
      "v_mfma_f32_16x16x32_bf16 v[128:131], v[8:11], a[136:139], v[128:131]\n"
      "ds_read_b128 a[148:151], v240 offset:4352\n"
      "v_cvt_pk_bf16_f32 v173, v138, v139\n"
      "v_mfma_f32_16x16x32_bf16 v[128:131], v[12:15], a[140:143], v[128:131]\n"
      "v_exp_f32_e32 v142, v142\n"
      "v_mfma_f32_16x16x32_bf16 v[132:135], v[28:31], a[140:143], 0\n"
      "ds_read_b128 a[152:155], v240 offset:4608\n"
      "v_cvt_pk_bf16_f32 v174, v140, v141\n"
      "v_mfma_f32_16x16x32_bf16 v[132:135], v[24:27], a[136:139], v[132:135]\n"
      "v_exp_f32_e32 v143, v143\n"
      "v_mfma_f32_16x16x32_bf16 v[132:135], v[20:23], a[132:135], v[132:135]\n"
      "ds_read_b128 a[156:159], v240 offset:4864\n"
      "v_cvt_pk_bf16_f32 v175, v142, v143\n"
      "v_mfma_f32_16x16x32_bf16 v[132:135], v[16:19], a[128:131], v[132:135]\n"
      "v_exp_f32_e32 v128, v128\n"
      "v_smfmac_f32_16x16x64_bf16 v[224:227], v[220:223], v[168:175], v241\n"
      "ds_write_b128 v242, v[172:175] offset:15360\n"
      "v_mfma_f32_16x16x32_bf16 v[192:195], a[0:3], v[144:147], 0\n"
      "v_mfma_f32_16x16x32_bf16 v[192:195], a[8:11], v[148:151], v[192:195]\n"
      "v_exp_f32_e32 v129, v129\n"
      "v_mfma_f32_16x16x32_bf16 v[192:195], a[16:19], v[152:155], v[192:195]\n"
      "v_mfma_f32_16x16x32_bf16 v[192:195], a[24:27], v[156:159], v[192:195]\n"
      "v_exp_f32_e32 v130, v130\n"
      "v_mfma_f32_16x16x32_bf16 v[196:199], a[28:31], v[156:159], 0\n"
      "s_waitcnt vmcnt(10) lgkmcnt(0)\n"
      "s_barrier\n"
      "v_mfma_f32_16x16x32_bf16 v[196:199], a[20:23], v[152:155], v[196:199]\n"
      "v_exp_f32_e32 v131, v131\n"
      "v_mfma_f32_16x16x32_bf16 v[196:199], a[12:15], v[148:151], v[196:199]\n"
      "v_add_f32_e32 v235, v224, v225\n"
      "v_mov_b32_e32 v224, s28\n"
      "v_mfma_f32_16x16x32_bf16 v[196:199], a[4:7], v[144:147], v[196:199]\n"
      "v_exp_f32_e32 v132, v132\n"
      "v_mfma_f32_16x16x32_bf16 v[136:139], v[0:3], a[144:147], 0\n"
      "ds_read_b128 a[160:163], v243 offset:8192\n"
      "v_cvt_pk_bf16_f32 v144, v128, v129\n"
      "v_mfma_f32_16x16x32_bf16 v[136:139], v[4:7], a[148:151], v[136:139]\n"
      "s_add_u32 s58, s58, 0x2000\n"
      "v_exp_f32_e32 v133, v133\n"
      "v_mfma_f32_16x16x32_bf16 v[136:139], v[8:11], a[152:155], v[136:139]\n"
      "ds_read_b128 a[164:167], v243 offset:10240\n"
      "v_cvt_pk_bf16_f32 v145, v130, v131\n"
      "v_mfma_f32_16x16x32_bf16 v[136:139], v[12:15], a[156:159], v[136:139]\n"
      "s_min_u32 s58, s58, s67\n"
      "v_exp_f32_e32 v134, v134\n"
      "v_mfma_f32_16x16x32_bf16 v[140:143], v[28:31], a[156:159], 0\n"
      "ds_read_b128 a[168:171], v243 offset:12288\n"
      "v_cvt_pk_bf16_f32 v146, v132, v133\n"
      "v_mfma_f32_16x16x32_bf16 v[140:143], v[24:27], a[152:155], v[140:143]\n"
      "s_xor_b32 m0, m0, 0x6000\n"
      "v_exp_f32_e32 v135, v135\n"
      "v_mfma_f32_16x16x32_bf16 v[140:143], v[20:23], a[148:151], v[140:143]\n"
      "ds_read_b128 a[172:175], v243 offset:14336\n"
      "v_cvt_pk_bf16_f32 v147, v134, v135\n"
      "v_mfma_f32_16x16x32_bf16 v[140:143], v[16:19], a[144:147], v[140:143]\n"
      "v_exp_f32_e32 v136, v136\n"
      "v_mfma_f32_16x16x32_bf16 v[200:203], a[0:3], v[160:163], 0\n"
      "ds_write_b128 v242, v[144:147] offset:0\n"
      "v_mfma_f32_16x16x32_bf16 v[200:203], a[8:11], v[164:167], v[200:203]\n"
      "v_exp_f32_e32 v137, v137\n"
      "v_mfma_f32_16x16x32_bf16 v[200:203], a[16:19], v[168:171], v[200:203]\n"
      "buffer_load_dwordx4 v248, s[40:43], s58 offen offset:0 lds\n"
      "v_mov_b32_e32 v225, 0\n"
      "v_mfma_f32_16x16x32_bf16 v[200:203], a[24:27], v[172:175], v[200:203]\n"
      "v_exp_f32_e32 v138, v138\n"
      "v_mfma_f32_16x16x32_bf16 v[204:207], a[28:31], v[172:175], 0\n"
      "ds_read_b128 v[228:231], v247 offset:0\n"
      "v_mfma_f32_16x16x32_bf16 v[204:207], a[20:23], v[168:171], v[204:207]\n"
      "v_exp_f32_e32 v139, v139\n"
      "v_mfma_f32_16x16x32_bf16 v[204:207], a[12:15], v[164:167], v[204:207]\n"
      "ds_write_b32 v246, v235 offset:1024\n"
      "v_mfma_f32_16x16x32_bf16 v[204:207], a[4:7], v[160:163], v[204:207]\n"
      "s_waitcnt lgkmcnt(3)\n"
      "v_exp_f32_e32 v140, v140\n"
      "v_mfma_f32_16x16x32_bf16 v[128:131], v[32:35], a[128:131], 0\n"
      "ds_read_b128 a[176:179], v243 offset:9216\n"
      "v_cvt_pk_bf16_f32 v160, v136, v137\n"
      "v_mfma_f32_16x16x32_bf16 v[128:131], v[36:39], a[132:135], v[128:131]\n"
      "v_exp_f32_e32 v141, v141\n"
      "v_mfma_f32_16x16x32_bf16 v[128:131], v[40:43], a[136:139], v[128:131]\n"
      "ds_read_b128 a[180:183], v243 offset:11264\n"
      "v_cvt_pk_bf16_f32 v161, v138, v139\n"
      "v_mfma_f32_16x16x32_bf16 v[128:131], v[44:47], a[140:143], v[128:131]\n"
      "v_exp_f32_e32 v142, v142\n"
      "v_mfma_f32_16x16x32_bf16 v[132:135], v[60:63], a[140:143], 0\n"
      "ds_read_b128 a[184:187], v243 offset:13312\n"
      "v_cvt_pk_bf16_f32 v162, v140, v141\n"
      "v_mfma_f32_16x16x32_bf16 v[132:135], v[56:59], a[136:139], v[132:135]\n"
      "v_exp_f32_e32 v143, v143\n"
      "v_mfma_f32_16x16x32_bf16 v[132:135], v[52:55], a[132:135], v[132:135]\n"
      "ds_read_b128 a[188:191], v243 offset:15360\n"
      "v_cvt_pk_bf16_f32 v163, v142, v143\n"
      "v_mfma_f32_16x16x32_bf16 v[132:135], v[48:51], a[128:131], v[132:135]\n"
      "s_waitcnt lgkmcnt(5)\n"
      "v_exp_f32_e32 v128, v128\n"
      "v_mfma_f32_16x16x32_bf16 v[192:195], a[32:35], a[160:163], v[192:195]\n"
      "ds_write_b128 v242, v[160:163] offset:1024\n"
      "v_mfma_f32_16x16x32_bf16 v[192:195], a[40:43], a[164:167], v[192:195]\n"
      "v_exp_f32_e32 v129, v129\n"
      "v_mfma_f32_16x16x32_bf16 v[192:195], a[48:51], a[168:171], v[192:195]\n"
      "buffer_load_dwordx4 v248, s[40:43], s58 offen offset:1024 lds\n"
      "v_add_f32_e32 v228, v228, v229\n"
      "v_mfma_f32_16x16x32_bf16 v[192:195], a[56:59], a[172:175], v[192:195]\n"
      "v_exp_f32_e32 v130, v130\n"
      "v_mfma_f32_16x16x32_bf16 v[196:199], a[60:63], a[172:175], v[196:199]\n"
      "v_add_f32_e32 v230, v230, v231\n"
      "v_add_f32_e32 v234, v228, v230\n"
      "v_mfma_f32_16x16x32_bf16 v[196:199], a[52:55], a[168:171], v[196:199]\n"
      "v_exp_f32_e32 v131, v131\n"
      "v_mfma_f32_16x16x32_bf16 v[196:199], a[44:47], a[164:167], v[196:199]\n"
      "v_rcp_f32_e32 v232, v234\n"
      "v_mfma_f32_16x16x32_bf16 v[196:199], a[36:39], a[160:163], v[196:199]\n"
      "s_waitcnt lgkmcnt(1)\n"
      "v_exp_f32_e32 v132, v132\n"
      "v_mfma_f32_16x16x32_bf16 v[136:139], v[32:35], a[144:147], 0\n"
      "ds_read_b128 a[160:163], v244 offset:8192\n"
      "v_cvt_pk_bf16_f32 v148, v128, v129\n"
      "v_mfma_f32_16x16x32_bf16 v[136:139], v[36:39], a[148:151], v[136:139]\n"
      "v_exp_f32_e32 v133, v133\n"
      "v_mfma_f32_16x16x32_bf16 v[136:139], v[40:43], a[152:155], v[136:139]\n"
      "ds_read_b128 a[164:167], v244 offset:10240\n"
      "v_cvt_pk_bf16_f32 v149, v130, v131\n"
      "v_mfma_f32_16x16x32_bf16 v[136:139], v[44:47], a[156:159], v[136:139]\n"
      "v_exp_f32_e32 v134, v134\n"
      "v_mfma_f32_16x16x32_bf16 v[140:143], v[60:63], a[156:159], 0\n"
      "ds_read_b128 a[168:171], v244 offset:12288\n"
      "v_cvt_pk_bf16_f32 v150, v132, v133\n"
      "v_mfma_f32_16x16x32_bf16 v[140:143], v[56:59], a[152:155], v[140:143]\n"
      "v_exp_f32_e32 v135, v135\n"
      "v_mfma_f32_16x16x32_bf16 v[140:143], v[52:55], a[148:151], v[140:143]\n"
      "ds_read_b128 a[172:175], v244 offset:14336\n"
      "v_cvt_pk_bf16_f32 v151, v134, v135\n"
      "v_mfma_f32_16x16x32_bf16 v[140:143], v[48:51], a[144:147], v[140:143]\n"
      "v_exp_f32_e32 v136, v136\n"
      "v_smfmac_f32_16x16x64_bf16 v[224:227], v[216:219], v[144:151], v241\n"
      "ds_write_b128 v242, v[148:151] offset:2048\n"
      "v_mfma_f32_16x16x32_bf16 v[200:203], a[32:35], a[176:179], v[200:203]\n"
      "s_add_u32 s59, s59, 0x2000\n"
      "v_mov_b32_e32 v233, v232\n"
      "v_med3_f32 v229, v234, 1.0, s63\n"
      "v_mfma_f32_16x16x32_bf16 v[200:203], a[40:43], a[180:183], v[200:203]\n"
      "v_exp_f32_e32 v137, v137\n"
      "v_mfma_f32_16x16x32_bf16 v[200:203], a[48:51], a[184:187], v[200:203]\n"
      "v_permlane32_swap_b32_e32 v232, v233\n"
      "v_mfma_f32_16x16x32_bf16 v[200:203], a[56:59], a[188:191], v[200:203]\n"
      "v_exp_f32_e32 v138, v138\n"
      "v_mfma_f32_16x16x32_bf16 v[204:207], a[60:63], a[188:191], v[204:207]\n"
      "v_cmp_neq_f32_e32 vcc, v229, v234\n"
      "v_mul_f32_e32 v176, v232, v176\n"
      "v_mfma_f32_16x16x32_bf16 v[204:207], a[52:55], a[184:187], v[204:207]\n"
      "v_exp_f32_e32 v139, v139\n"
      "v_mfma_f32_16x16x32_bf16 v[204:207], a[44:47], a[180:183], v[204:207]\n"
      "v_mul_f32_e32 v177, v232, v177\n"
      "v_mul_f32_e32 v178, v232, v178\n"
      "v_mfma_f32_16x16x32_bf16 v[204:207], a[36:39], a[176:179], v[204:207]\n"
      "s_waitcnt lgkmcnt(1)\n"
      "v_exp_f32_e32 v140, v140\n"
      "v_mfma_f32_16x16x32_bf16 v[128:131], v[64:67], a[128:131], 0\n"
      "ds_read_b128 a[176:179], v244 offset:9216\n"
      "v_cvt_pk_bf16_f32 v164, v136, v137\n"
      "v_mfma_f32_16x16x32_bf16 v[128:131], v[68:71], a[132:135], v[128:131]\n"
      "s_cmp_lg_u64 vcc, 0\n"
      "v_exp_f32_e32 v141, v141\n"
      "v_mfma_f32_16x16x32_bf16 v[128:131], v[72:75], a[136:139], v[128:131]\n"
      "ds_read_b128 a[180:183], v244 offset:11264\n"
      "v_cvt_pk_bf16_f32 v165, v138, v139\n"
      "v_mfma_f32_16x16x32_bf16 v[128:131], v[76:79], a[140:143], v[128:131]\n"
      "s_addc_u32 s66, s66, s66\n"
      "v_exp_f32_e32 v142, v142\n"
      "v_mfma_f32_16x16x32_bf16 v[132:135], v[92:95], a[140:143], 0\n"
      "ds_read_b128 a[184:187], v244 offset:13312\n"
      "v_cvt_pk_bf16_f32 v166, v140, v141\n"
      "v_mfma_f32_16x16x32_bf16 v[132:135], v[88:91], a[136:139], v[132:135]\n"
      "v_exp_f32_e32 v143, v143\n"
      "v_mfma_f32_16x16x32_bf16 v[132:135], v[84:87], a[132:135], v[132:135]\n"
      "ds_read_b128 a[188:191], v244 offset:15360\n"
      "v_cvt_pk_bf16_f32 v167, v142, v143\n"
      "v_mfma_f32_16x16x32_bf16 v[132:135], v[80:83], a[128:131], v[132:135]\n"
      "v_exp_f32_e32 v128, v128\n"
      "v_smfmac_f32_16x16x64_bf16 v[224:227], v[220:223], v[160:167], v241\n"
      "ds_write_b128 v242, v[164:167] offset:3072\n"
      "v_mfma_f32_16x16x32_bf16 v[192:195], a[64:67], a[160:163], v[192:195]\n"
      "v_mul_f32_e32 v179, v232, v179\n"
      "v_cvt_pk_bf16_f32 v208, v176, v177\n"
      "v_mfma_f32_16x16x32_bf16 v[192:195], a[72:75], a[164:167], v[192:195]\n"
      "v_exp_f32_e32 v129, v129\n"
      "v_mfma_f32_16x16x32_bf16 v[192:195], a[80:83], a[168:171], v[192:195]\n"
      "v_cvt_pk_bf16_f32 v209, v178, v179\n"
      "v_mfma_f32_16x16x32_bf16 v[192:195], a[88:91], a[172:175], v[192:195]\n"
      "v_exp_f32_e32 v130, v130\n"
      "v_mfma_f32_16x16x32_bf16 v[196:199], a[92:95], a[172:175], v[196:199]\n"
      "v_mul_f32_e32 v180, v232, v180\n"
      "v_mul_f32_e32 v181, v232, v181\n"
      "v_mfma_f32_16x16x32_bf16 v[196:199], a[84:87], a[168:171], v[196:199]\n"
      "v_exp_f32_e32 v131, v131\n"
      "v_mfma_f32_16x16x32_bf16 v[196:199], a[76:79], a[164:167], v[196:199]\n"
      "v_mul_f32_e32 v182, v232, v182\n"
      "v_mul_f32_e32 v183, v232, v183\n"
      "v_mfma_f32_16x16x32_bf16 v[196:199], a[68:71], a[160:163], v[196:199]\n"
      "s_waitcnt lgkmcnt(1)\n"
      "v_exp_f32_e32 v132, v132\n"
      "v_mfma_f32_16x16x32_bf16 v[136:139], v[64:67], a[144:147], 0\n"
      "ds_read_b128 a[160:163], v245 offset:8192\n"
      "v_cvt_pk_bf16_f32 v152, v128, v129\n"
      "v_mfma_f32_16x16x32_bf16 v[136:139], v[68:71], a[148:151], v[136:139]\n"
      "v_exp_f32_e32 v133, v133\n"
      "v_mfma_f32_16x16x32_bf16 v[136:139], v[72:75], a[152:155], v[136:139]\n"
      "ds_read_b128 a[164:167], v245 offset:10240\n"
      "v_cvt_pk_bf16_f32 v153, v130, v131\n"
      "v_mfma_f32_16x16x32_bf16 v[136:139], v[76:79], a[156:159], v[136:139]\n"
      "v_exp_f32_e32 v134, v134\n"
      "v_mfma_f32_16x16x32_bf16 v[140:143], v[92:95], a[156:159], 0\n"
      "ds_read_b128 a[168:171], v245 offset:12288\n"
      "v_cvt_pk_bf16_f32 v154, v132, v133\n"
      "v_mfma_f32_16x16x32_bf16 v[140:143], v[88:91], a[152:155], v[140:143]\n"
      "v_exp_f32_e32 v135, v135\n"
      "v_mfma_f32_16x16x32_bf16 v[140:143], v[84:87], a[148:151], v[140:143]\n"
      "ds_read_b128 a[172:175], v245 offset:14336\n"
      "v_cvt_pk_bf16_f32 v155, v134, v135\n"
      "v_mfma_f32_16x16x32_bf16 v[140:143], v[80:83], a[144:147], v[140:143]\n"
      "v_exp_f32_e32 v136, v136\n"
      "v_mfma_f32_16x16x32_bf16 v[200:203], a[64:67], a[176:179], v[200:203]\n"
      "ds_write_b128 v242, v[152:155] offset:4096\n"
      "v_mfma_f32_16x16x32_bf16 v[200:203], a[72:75], a[180:183], v[200:203]\n"
      "v_exp_f32_e32 v137, v137\n"
      "v_mfma_f32_16x16x32_bf16 v[200:203], a[80:83], a[184:187], v[200:203]\n"
      "v_cvt_pk_bf16_f32 v210, v180, v181\n"
      "v_mfma_f32_16x16x32_bf16 v[200:203], a[88:91], a[188:191], v[200:203]\n"
      "v_exp_f32_e32 v138, v138\n"
      "v_mfma_f32_16x16x32_bf16 v[204:207], a[92:95], a[188:191], v[204:207]\n"
      "v_cvt_pk_bf16_f32 v211, v182, v183\n"
      "v_mfma_f32_16x16x32_bf16 v[204:207], a[84:87], a[184:187], v[204:207]\n"
      "v_exp_f32_e32 v139, v139\n"
      "v_mfma_f32_16x16x32_bf16 v[204:207], a[76:79], a[180:183], v[204:207]\n"
      "buffer_store_dwordx4 v[208:211], v249, s[52:55], s59 offen offset:0 sc0 sc1\n"
      "v_mfma_f32_16x16x32_bf16 v[204:207], a[68:71], a[176:179], v[204:207]\n"
      "s_waitcnt lgkmcnt(1)\n"
      "v_exp_f32_e32 v140, v140\n"
      "v_mfma_f32_16x16x32_bf16 v[128:131], v[96:99], a[128:131], 0\n"
      "ds_read_b128 a[176:179], v245 offset:9216\n"
      "v_cvt_pk_bf16_f32 v168, v136, v137\n"
      "v_mfma_f32_16x16x32_bf16 v[128:131], v[100:103], a[132:135], v[128:131]\n"
      "v_exp_f32_e32 v141, v141\n"
      "v_mfma_f32_16x16x32_bf16 v[128:131], v[104:107], a[136:139], v[128:131]\n"
      "ds_read_b128 a[180:183], v245 offset:11264\n"
      "v_cvt_pk_bf16_f32 v169, v138, v139\n"
      "v_mfma_f32_16x16x32_bf16 v[128:131], v[108:111], a[140:143], v[128:131]\n"
      "v_exp_f32_e32 v142, v142\n"
      "v_mfma_f32_16x16x32_bf16 v[132:135], v[124:127], a[140:143], 0\n"
      "ds_read_b128 a[184:187], v245 offset:13312\n"
      "v_cvt_pk_bf16_f32 v170, v140, v141\n"
      "v_mfma_f32_16x16x32_bf16 v[132:135], v[120:123], a[136:139], v[132:135]\n"
      "v_exp_f32_e32 v143, v143\n"
      "v_mfma_f32_16x16x32_bf16 v[132:135], v[116:119], a[132:135], v[132:135]\n"
      "ds_read_b128 a[188:191], v245 offset:15360\n"
      "v_cvt_pk_bf16_f32 v171, v142, v143\n"
      "v_mfma_f32_16x16x32_bf16 v[132:135], v[112:115], a[128:131], v[132:135]\n"
      "v_exp_f32_e32 v128, v128\n"
      "v_mfma_f32_16x16x32_bf16 v[192:195], a[96:99], a[160:163], v[192:195]\n"
      "ds_write_b128 v242, v[168:171] offset:5120\n"
      "v_mfma_f32_16x16x32_bf16 v[192:195], a[104:107], a[164:167], v[192:195]\n"
      "v_exp_f32_e32 v129, v129\n"
      "v_mfma_f32_16x16x32_bf16 v[192:195], a[112:115], a[168:171], v[192:195]\n"
      "v_mul_f32_e32 v184, v233, v184\n"
      "v_mul_f32_e32 v185, v233, v185\n"
      "v_mfma_f32_16x16x32_bf16 v[192:195], a[120:123], a[172:175], v[192:195]\n"
      "v_exp_f32_e32 v130, v130\n"
      "v_mfma_f32_16x16x32_bf16 v[196:199], a[124:127], a[172:175], v[196:199]\n"
      "v_mul_f32_e32 v186, v233, v186\n"
      "v_mul_f32_e32 v187, v233, v187\n"
      "v_mfma_f32_16x16x32_bf16 v[196:199], a[116:119], a[168:171], v[196:199]\n"
      "v_exp_f32_e32 v131, v131\n"
      "v_mfma_f32_16x16x32_bf16 v[196:199], a[108:111], a[164:167], v[196:199]\n"
      "v_cvt_pk_bf16_f32 v212, v184, v185\n"
      "v_cvt_pk_bf16_f32 v213, v186, v187\n"
      "v_mfma_f32_16x16x32_bf16 v[196:199], a[100:103], a[160:163], v[196:199]\n"
      "s_waitcnt lgkmcnt(1)\n"
      "v_exp_f32_e32 v132, v132\n"
      "v_mfma_f32_16x16x32_bf16 v[136:139], v[96:99], a[144:147], 0\n"
      "ds_read_b128 a[128:131], v240 offset:8192\n"
      "v_cvt_pk_bf16_f32 v156, v128, v129\n"
      "v_mfma_f32_16x16x32_bf16 v[136:139], v[100:103], a[148:151], v[136:139]\n"
      "v_exp_f32_e32 v133, v133\n"
      "v_mfma_f32_16x16x32_bf16 v[136:139], v[104:107], a[152:155], v[136:139]\n"
      "ds_read_b128 a[132:135], v240 offset:8448\n"
      "v_cvt_pk_bf16_f32 v157, v130, v131\n"
      "v_mfma_f32_16x16x32_bf16 v[136:139], v[108:111], a[156:159], v[136:139]\n"
      "v_exp_f32_e32 v134, v134\n"
      "v_mfma_f32_16x16x32_bf16 v[140:143], v[124:127], a[156:159], 0\n"
      "ds_read_b128 a[136:139], v240 offset:8704\n"
      "v_cvt_pk_bf16_f32 v158, v132, v133\n"
      "v_mfma_f32_16x16x32_bf16 v[140:143], v[120:123], a[152:155], v[140:143]\n"
      "v_exp_f32_e32 v135, v135\n"
      "v_mfma_f32_16x16x32_bf16 v[140:143], v[116:119], a[148:151], v[140:143]\n"
      "ds_read_b128 a[140:143], v240 offset:8960\n"
      "v_cvt_pk_bf16_f32 v159, v134, v135\n"
      "v_mfma_f32_16x16x32_bf16 v[140:143], v[112:115], a[144:147], v[140:143]\n"
      "v_exp_f32_e32 v136, v136\n"
      "v_smfmac_f32_16x16x64_bf16 v[224:227], v[216:219], v[152:159], v241\n"
      "ds_write_b128 v242, v[156:159] offset:6144\n"
      "v_mfma_f32_16x16x32_bf16 v[200:203], a[96:99], a[176:179], v[200:203]\n"
      "v_mul_f32_e32 v188, v233, v188\n"
      "v_mul_f32_e32 v189, v233, v189\n"
      "v_mfma_f32_16x16x32_bf16 v[200:203], a[104:107], a[180:183], v[200:203]\n"
      "v_exp_f32_e32 v137, v137\n"
      "v_mfma_f32_16x16x32_bf16 v[200:203], a[112:115], a[184:187], v[200:203]\n"
      "v_mul_f32_e32 v190, v233, v190\n"
      "v_mul_f32_e32 v191, v233, v191\n"
      "v_mfma_f32_16x16x32_bf16 v[200:203], a[120:123], a[188:191], v[200:203]\n"
      "s_sub_u32 s61, s61, 1\n"
      "v_exp_f32_e32 v138, v138\n"
      "v_mfma_f32_16x16x32_bf16 v[204:207], a[124:127], a[188:191], v[204:207]\n"
      "v_cvt_pk_bf16_f32 v214, v188, v189\n"
      "v_cvt_pk_bf16_f32 v215, v190, v191\n"
      "v_mfma_f32_16x16x32_bf16 v[204:207], a[116:119], a[184:187], v[204:207]\n"
      "s_cmp_lg_u32 s61, 0\n"
      "v_exp_f32_e32 v139, v139\n"
      "v_mfma_f32_16x16x32_bf16 v[204:207], a[108:111], a[180:183], v[204:207]\n"
      "buffer_store_dwordx4 v[212:215], v250, s[52:55], s59 offen offset:0 sc0 sc1\n"
      "v_mfma_f32_16x16x32_bf16 v[204:207], a[100:103], a[176:179], v[204:207]\n"
      "s_waitcnt lgkmcnt(1)\n"
      "v_exp_f32_e32 v140, v140\n"
      "s_cbranch_scc1 .Lloop0_%=\n"
      "s_branch .Ltail_%=\n"
      ".p2align 6\n"
      ".Lloop1_%=:\n"
      "v_mfma_f32_16x16x32_bf16 v[128:131], v[0:3], a[128:131], 0\n"
      "ds_read_b128 a[144:147], v240 offset:12288\n"
      "v_cvt_pk_bf16_f32 v172, v136, v137\n"
      "v_mfma_f32_16x16x32_bf16 v[128:131], v[4:7], a[132:135], v[128:131]\n"
      "v_exp_f32_e32 v141, v141\n"
      "v_mfma_f32_16x16x32_bf16 v[128:131], v[8:11], a[136:139], v[128:131]\n"
      "ds_read_b128 a[148:151], v240 offset:12544\n"
      "v_cvt_pk_bf16_f32 v173, v138, v139\n"
      "v_mfma_f32_16x16x32_bf16 v[128:131], v[12:15], a[140:143], v[128:131]\n"
      "v_exp_f32_e32 v142, v142\n"
      "v_mfma_f32_16x16x32_bf16 v[132:135], v[28:31], a[140:143], 0\n"
      "ds_read_b128 a[152:155], v240 offset:12800\n"
      "v_cvt_pk_bf16_f32 v174, v140, v141\n"
      "v_mfma_f32_16x16x32_bf16 v[132:135], v[24:27], a[136:139], v[132:135]\n"
      "v_exp_f32_e32 v143, v143\n"
      "v_mfma_f32_16x16x32_bf16 v[132:135], v[20:23], a[132:135], v[132:135]\n"
      "ds_read_b128 a[156:159], v240 offset:13056\n"
      "v_cvt_pk_bf16_f32 v175, v142, v143\n"
      "v_mfma_f32_16x16x32_bf16 v[132:135], v[16:19], a[128:131], v[132:135]\n"
      "v_exp_f32_e32 v128, v128\n"
      "v_smfmac_f32_16x16x64_bf16 v[224:227], v[220:223], v[168:175], v241\n"
      "ds_write_b128 v242, v[172:175] offset:7168\n"
      "v_mfma_f32_16x16x32_bf16 v[176:179], a[0:3], v[144:147], 0\n"
      "v_xor_b32_e32 v240, 0x4000, v240\n"
      "v_mfma_f32_16x16x32_bf16 v[176:179], a[8:11], v[148:151], v[176:179]\n"
      "v_exp_f32_e32 v129, v129\n"
      "v_mfma_f32_16x16x32_bf16 v[176:179], a[16:19], v[152:155], v[176:179]\n"
      "v_mfma_f32_16x16x32_bf16 v[176:179], a[24:27], v[156:159], v[176:179]\n"
      "v_exp_f32_e32 v130, v130\n"
      "v_mfma_f32_16x16x32_bf16 v[180:183], a[28:31], v[156:159], 0\n"
      "v_add_f32_e32 v235, v224, v225\n"
      "v_mov_b32_e32 v224, s28\n"
      "v_mfma_f32_16x16x32_bf16 v[180:183], a[20:23], v[152:155], v[180:183]\n"
      "v_exp_f32_e32 v131, v131\n"
      "v_mfma_f32_16x16x32_bf16 v[180:183], a[12:15], v[148:151], v[180:183]\n"
      "s_waitcnt vmcnt(10) lgkmcnt(0)\n"
      "s_barrier\n"
      "v_mfma_f32_16x16x32_bf16 v[180:183], a[4:7], v[144:147], v[180:183]\n"
      "v_exp_f32_e32 v132, v132\n"
      "v_mfma_f32_16x16x32_bf16 v[136:139], v[0:3], a[144:147], 0\n"
      "ds_read_b128 a[160:163], v243 offset:0\n"
      "v_cvt_pk_bf16_f32 v144, v128, v129\n"
      "v_mfma_f32_16x16x32_bf16 v[136:139], v[4:7], a[148:151], v[136:139]\n"
      "s_add_u32 s58, s58, 0x2000\n"
      "v_exp_f32_e32 v133, v133\n"
      "v_mfma_f32_16x16x32_bf16 v[136:139], v[8:11], a[152:155], v[136:139]\n"
      "ds_read_b128 a[164:167], v243 offset:2048\n"
      "v_cvt_pk_bf16_f32 v145, v130, v131\n"
      "v_mfma_f32_16x16x32_bf16 v[136:139], v[12:15], a[156:159], v[136:139]\n"
      "s_min_u32 s58, s58, s67\n"
      "v_exp_f32_e32 v134, v134\n"
      "v_mfma_f32_16x16x32_bf16 v[140:143], v[28:31], a[156:159], 0\n"
      "ds_read_b128 a[168:171], v243 offset:4096\n"
      "v_cvt_pk_bf16_f32 v146, v132, v133\n"
      "v_mfma_f32_16x16x32_bf16 v[140:143], v[24:27], a[152:155], v[140:143]\n"
      "s_xor_b32 m0, m0, 0x2000\n"
      "v_exp_f32_e32 v135, v135\n"
      "v_mfma_f32_16x16x32_bf16 v[140:143], v[20:23], a[148:151], v[140:143]\n"
      "ds_read_b128 a[172:175], v243 offset:6144\n"
      "v_cvt_pk_bf16_f32 v147, v134, v135\n"
      "v_mfma_f32_16x16x32_bf16 v[140:143], v[16:19], a[144:147], v[140:143]\n"
      "v_exp_f32_e32 v136, v136\n"
      "v_mfma_f32_16x16x32_bf16 v[184:187], a[0:3], v[160:163], 0\n"
      "ds_write_b128 v242, v[144:147] offset:8192\n"
      "v_mfma_f32_16x16x32_bf16 v[184:187], a[8:11], v[164:167], v[184:187]\n"
      "v_exp_f32_e32 v137, v137\n"
      "v_mfma_f32_16x16x32_bf16 v[184:187], a[16:19], v[168:171], v[184:187]\n"
      "buffer_load_dwordx4 v248, s[40:43], s58 offen offset:0 lds\n"
      "v_mov_b32_e32 v225, 0\n"
      "v_mfma_f32_16x16x32_bf16 v[184:187], a[24:27], v[172:175], v[184:187]\n"
      "v_exp_f32_e32 v138, v138\n"
      "v_mfma_f32_16x16x32_bf16 v[188:191], a[28:31], v[172:175], 0\n"
      "ds_read_b128 v[228:231], v247 offset:1024\n"
      "v_mfma_f32_16x16x32_bf16 v[188:191], a[20:23], v[168:171], v[188:191]\n"
      "v_exp_f32_e32 v139, v139\n"
      "v_mfma_f32_16x16x32_bf16 v[188:191], a[12:15], v[164:167], v[188:191]\n"
      "ds_write_b32 v246, v235 offset:0\n"
      "v_mfma_f32_16x16x32_bf16 v[188:191], a[4:7], v[160:163], v[188:191]\n"
      "s_waitcnt lgkmcnt(3)\n"
      "v_exp_f32_e32 v140, v140\n"
      "v_mfma_f32_16x16x32_bf16 v[128:131], v[32:35], a[128:131], 0\n"
      "ds_read_b128 a[176:179], v243 offset:1024\n"
      "v_cvt_pk_bf16_f32 v160, v136, v137\n"
      "v_mfma_f32_16x16x32_bf16 v[128:131], v[36:39], a[132:135], v[128:131]\n"
      "v_exp_f32_e32 v141, v141\n"
      "v_mfma_f32_16x16x32_bf16 v[128:131], v[40:43], a[136:139], v[128:131]\n"
      "ds_read_b128 a[180:183], v243 offset:3072\n"
      "v_cvt_pk_bf16_f32 v161, v138, v139\n"
      "v_mfma_f32_16x16x32_bf16 v[128:131], v[44:47], a[140:143], v[128:131]\n"
      "v_exp_f32_e32 v142, v142\n"
      "v_mfma_f32_16x16x32_bf16 v[132:135], v[60:63], a[140:143], 0\n"
      "ds_read_b128 a[184:187], v243 offset:5120\n"
      "v_cvt_pk_bf16_f32 v162, v140, v141\n"
      "v_mfma_f32_16x16x32_bf16 v[132:135], v[56:59], a[136:139], v[132:135]\n"
      "v_exp_f32_e32 v143, v143\n"
      "v_mfma_f32_16x16x32_bf16 v[132:135], v[52:55], a[132:135], v[132:135]\n"
      "ds_read_b128 a[188:191], v243 offset:7168\n"
      "v_cvt_pk_bf16_f32 v163, v142, v143\n"
      "v_mfma_f32_16x16x32_bf16 v[132:135], v[48:51], a[128:131], v[132:135]\n"
      "s_waitcnt lgkmcnt(5)\n"
      "v_exp_f32_e32 v128, v128\n"
      "v_mfma_f32_16x16x32_bf16 v[176:179], a[32:35], a[160:163], v[176:179]\n"
      "ds_write_b128 v242, v[160:163] offset:9216\n"
      "v_mfma_f32_16x16x32_bf16 v[176:179], a[40:43], a[164:167], v[176:179]\n"
      "v_exp_f32_e32 v129, v129\n"
      "v_mfma_f32_16x16x32_bf16 v[176:179], a[48:51], a[168:171], v[176:179]\n"
      "buffer_load_dwordx4 v248, s[40:43], s58 offen offset:1024 lds\n"
      "v_add_f32_e32 v228, v228, v229\n"
      "v_mfma_f32_16x16x32_bf16 v[176:179], a[56:59], a[172:175], v[176:179]\n"
      "v_exp_f32_e32 v130, v130\n"
      "v_mfma_f32_16x16x32_bf16 v[180:183], a[60:63], a[172:175], v[180:183]\n"
      "v_add_f32_e32 v230, v230, v231\n"
      "v_add_f32_e32 v234, v228, v230\n"
      "v_mfma_f32_16x16x32_bf16 v[180:183], a[52:55], a[168:171], v[180:183]\n"
      "v_exp_f32_e32 v131, v131\n"
      "v_mfma_f32_16x16x32_bf16 v[180:183], a[44:47], a[164:167], v[180:183]\n"
      "v_rcp_f32_e32 v232, v234\n"
      "v_mfma_f32_16x16x32_bf16 v[180:183], a[36:39], a[160:163], v[180:183]\n"
      "s_waitcnt lgkmcnt(1)\n"
      "v_exp_f32_e32 v132, v132\n"
      "v_mfma_f32_16x16x32_bf16 v[136:139], v[32:35], a[144:147], 0\n"
      "ds_read_b128 a[160:163], v244 offset:0\n"
      "v_cvt_pk_bf16_f32 v148, v128, v129\n"
      "v_mfma_f32_16x16x32_bf16 v[136:139], v[36:39], a[148:151], v[136:139]\n"
      "v_exp_f32_e32 v133, v133\n"
      "v_mfma_f32_16x16x32_bf16 v[136:139], v[40:43], a[152:155], v[136:139]\n"
      "ds_read_b128 a[164:167], v244 offset:2048\n"
      "v_cvt_pk_bf16_f32 v149, v130, v131\n"
      "v_mfma_f32_16x16x32_bf16 v[136:139], v[44:47], a[156:159], v[136:139]\n"
      "v_exp_f32_e32 v134, v134\n"
      "v_mfma_f32_16x16x32_bf16 v[140:143], v[60:63], a[156:159], 0\n"
      "ds_read_b128 a[168:171], v244 offset:4096\n"
      "v_cvt_pk_bf16_f32 v150, v132, v133\n"
      "v_mfma_f32_16x16x32_bf16 v[140:143], v[56:59], a[152:155], v[140:143]\n"
      "v_exp_f32_e32 v135, v135\n"
      "v_mfma_f32_16x16x32_bf16 v[140:143], v[52:55], a[148:151], v[140:143]\n"
      "ds_read_b128 a[172:175], v244 offset:6144\n"
      "v_cvt_pk_bf16_f32 v151, v134, v135\n"
      "v_mfma_f32_16x16x32_bf16 v[140:143], v[48:51], a[144:147], v[140:143]\n"
      "v_exp_f32_e32 v136, v136\n"
      "v_smfmac_f32_16x16x64_bf16 v[224:227], v[216:219], v[144:151], v241\n"
      "ds_write_b128 v242, v[148:151] offset:10240\n"
      "v_mfma_f32_16x16x32_bf16 v[184:187], a[32:35], a[176:179], v[184:187]\n"
      "s_add_u32 s59, s59, 0x2000\n"
      "v_mov_b32_e32 v233, v232\n"
      "v_med3_f32 v229, v234, 1.0, s63\n"
      "v_mfma_f32_16x16x32_bf16 v[184:187], a[40:43], a[180:183], v[184:187]\n"
      "v_exp_f32_e32 v137, v137\n"
      "v_mfma_f32_16x16x32_bf16 v[184:187], a[48:51], a[184:187], v[184:187]\n"
      "v_permlane32_swap_b32_e32 v232, v233\n"
      "v_mfma_f32_16x16x32_bf16 v[184:187], a[56:59], a[188:191], v[184:187]\n"
      "v_exp_f32_e32 v138, v138\n"
      "v_mfma_f32_16x16x32_bf16 v[188:191], a[60:63], a[188:191], v[188:191]\n"
      "v_cmp_neq_f32_e32 vcc, v229, v234\n"
      "v_mul_f32_e32 v192, v232, v192\n"
      "v_mfma_f32_16x16x32_bf16 v[188:191], a[52:55], a[184:187], v[188:191]\n"
      "v_exp_f32_e32 v139, v139\n"
      "v_mfma_f32_16x16x32_bf16 v[188:191], a[44:47], a[180:183], v[188:191]\n"
      "v_mul_f32_e32 v193, v232, v193\n"
      "v_mul_f32_e32 v194, v232, v194\n"
      "v_mfma_f32_16x16x32_bf16 v[188:191], a[36:39], a[176:179], v[188:191]\n"
      "s_waitcnt lgkmcnt(1)\n"
      "v_exp_f32_e32 v140, v140\n"
      "v_mfma_f32_16x16x32_bf16 v[128:131], v[64:67], a[128:131], 0\n"
      "ds_read_b128 a[176:179], v244 offset:1024\n"
      "v_cvt_pk_bf16_f32 v164, v136, v137\n"
      "v_mfma_f32_16x16x32_bf16 v[128:131], v[68:71], a[132:135], v[128:131]\n"
      "s_cmp_lg_u64 vcc, 0\n"
      "v_exp_f32_e32 v141, v141\n"
      "v_mfma_f32_16x16x32_bf16 v[128:131], v[72:75], a[136:139], v[128:131]\n"
      "ds_read_b128 a[180:183], v244 offset:3072\n"
      "v_cvt_pk_bf16_f32 v165, v138, v139\n"
      "v_mfma_f32_16x16x32_bf16 v[128:131], v[76:79], a[140:143], v[128:131]\n"
      "s_addc_u32 s66, s66, s66\n"
      "v_exp_f32_e32 v142, v142\n"
      "v_mfma_f32_16x16x32_bf16 v[132:135], v[92:95], a[140:143], 0\n"
      "ds_read_b128 a[184:187], v244 offset:5120\n"
      "v_cvt_pk_bf16_f32 v166, v140, v141\n"
      "v_mfma_f32_16x16x32_bf16 v[132:135], v[88:91], a[136:139], v[132:135]\n"
      "v_exp_f32_e32 v143, v143\n"
      "v_mfma_f32_16x16x32_bf16 v[132:135], v[84:87], a[132:135], v[132:135]\n"
      "ds_read_b128 a[188:191], v244 offset:7168\n"
      "v_cvt_pk_bf16_f32 v167, v142, v143\n"
      "v_mfma_f32_16x16x32_bf16 v[132:135], v[80:83], a[128:131], v[132:135]\n"
      "v_exp_f32_e32 v128, v128\n"
      "v_smfmac_f32_16x16x64_bf16 v[224:227], v[220:223], v[160:167], v241\n"
      "ds_write_b128 v242, v[164:167] offset:11264\n"
      "v_mfma_f32_16x16x32_bf16 v[176:179], a[64:67], a[160:163], v[176:179]\n"
      "v_mul_f32_e32 v195, v232, v195\n"
      "v_cvt_pk_bf16_f32 v208, v192, v193\n"
      "v_mfma_f32_16x16x32_bf16 v[176:179], a[72:75], a[164:167], v[176:179]\n"
      "v_exp_f32_e32 v129, v129\n"
      "v_mfma_f32_16x16x32_bf16 v[176:179], a[80:83], a[168:171], v[176:179]\n"
      "v_cvt_pk_bf16_f32 v209, v194, v195\n"
      "v_mfma_f32_16x16x32_bf16 v[176:179], a[88:91], a[172:175], v[176:179]\n"
      "v_exp_f32_e32 v130, v130\n"
      "v_mfma_f32_16x16x32_bf16 v[180:183], a[92:95], a[172:175], v[180:183]\n"
      "v_mul_f32_e32 v196, v232, v196\n"
      "v_mul_f32_e32 v197, v232, v197\n"
      "v_mfma_f32_16x16x32_bf16 v[180:183], a[84:87], a[168:171], v[180:183]\n"
      "v_exp_f32_e32 v131, v131\n"
      "v_mfma_f32_16x16x32_bf16 v[180:183], a[76:79], a[164:167], v[180:183]\n"
      "v_mul_f32_e32 v198, v232, v198\n"
      "v_mul_f32_e32 v199, v232, v199\n"
      "v_mfma_f32_16x16x32_bf16 v[180:183], a[68:71], a[160:163], v[180:183]\n"
      "s_waitcnt lgkmcnt(1)\n"
      "v_exp_f32_e32 v132, v132\n"
      "v_mfma_f32_16x16x32_bf16 v[136:139], v[64:67], a[144:147], 0\n"
      "ds_read_b128 a[160:163], v245 offset:0\n"
      "v_cvt_pk_bf16_f32 v152, v128, v129\n"
      "v_mfma_f32_16x16x32_bf16 v[136:139], v[68:71], a[148:151], v[136:139]\n"
      "v_exp_f32_e32 v133, v133\n"
      "v_mfma_f32_16x16x32_bf16 v[136:139], v[72:75], a[152:155], v[136:139]\n"
      "ds_read_b128 a[164:167], v245 offset:2048\n"
      "v_cvt_pk_bf16_f32 v153, v130, v131\n"
      "v_mfma_f32_16x16x32_bf16 v[136:139], v[76:79], a[156:159], v[136:139]\n"
      "v_exp_f32_e32 v134, v134\n"
      "v_mfma_f32_16x16x32_bf16 v[140:143], v[92:95], a[156:159], 0\n"
      "ds_read_b128 a[168:171], v245 offset:4096\n"
      "v_cvt_pk_bf16_f32 v154, v132, v133\n"
      "v_mfma_f32_16x16x32_bf16 v[140:143], v[88:91], a[152:155], v[140:143]\n"
      "v_exp_f32_e32 v135, v135\n"
      "v_mfma_f32_16x16x32_bf16 v[140:143], v[84:87], a[148:151], v[140:143]\n"
      "ds_read_b128 a[172:175], v245 offset:6144\n"
      "v_cvt_pk_bf16_f32 v155, v134, v135\n"
      "v_mfma_f32_16x16x32_bf16 v[140:143], v[80:83], a[144:147], v[140:143]\n"
      "v_exp_f32_e32 v136, v136\n"
      "v_mfma_f32_16x16x32_bf16 v[184:187], a[64:67], a[176:179], v[184:187]\n"
      "ds_write_b128 v242, v[152:155] offset:12288\n"
      "v_mfma_f32_16x16x32_bf16 v[184:187], a[72:75], a[180:183], v[184:187]\n"
      "v_exp_f32_e32 v137, v137\n"
      "v_mfma_f32_16x16x32_bf16 v[184:187], a[80:83], a[184:187], v[184:187]\n"
      "v_cvt_pk_bf16_f32 v210, v196, v197\n"
      "v_mfma_f32_16x16x32_bf16 v[184:187], a[88:91], a[188:191], v[184:187]\n"
      "v_exp_f32_e32 v138, v138\n"
      "v_mfma_f32_16x16x32_bf16 v[188:191], a[92:95], a[188:191], v[188:191]\n"
      "v_cvt_pk_bf16_f32 v211, v198, v199\n"
      "v_mfma_f32_16x16x32_bf16 v[188:191], a[84:87], a[184:187], v[188:191]\n"
      "v_exp_f32_e32 v139, v139\n"
      "v_mfma_f32_16x16x32_bf16 v[188:191], a[76:79], a[180:183], v[188:191]\n"
      "buffer_store_dwordx4 v[208:211], v249, s[52:55], s59 offen offset:0 sc0 sc1\n"
      "v_mfma_f32_16x16x32_bf16 v[188:191], a[68:71], a[176:179], v[188:191]\n"
      "s_waitcnt lgkmcnt(1)\n"
      "v_exp_f32_e32 v140, v140\n"
      "v_mfma_f32_16x16x32_bf16 v[128:131], v[96:99], a[128:131], 0\n"
      "ds_read_b128 a[176:179], v245 offset:1024\n"
      "v_cvt_pk_bf16_f32 v168, v136, v137\n"
      "v_mfma_f32_16x16x32_bf16 v[128:131], v[100:103], a[132:135], v[128:131]\n"
      "v_exp_f32_e32 v141, v141\n"
      "v_mfma_f32_16x16x32_bf16 v[128:131], v[104:107], a[136:139], v[128:131]\n"
      "ds_read_b128 a[180:183], v245 offset:3072\n"
      "v_cvt_pk_bf16_f32 v169, v138, v139\n"
      "v_mfma_f32_16x16x32_bf16 v[128:131], v[108:111], a[140:143], v[128:131]\n"
      "v_exp_f32_e32 v142, v142\n"
      "v_mfma_f32_16x16x32_bf16 v[132:135], v[124:127], a[140:143], 0\n"
      "ds_read_b128 a[184:187], v245 offset:5120\n"
      "v_cvt_pk_bf16_f32 v170, v140, v141\n"
      "v_mfma_f32_16x16x32_bf16 v[132:135], v[120:123], a[136:139], v[132:135]\n"
      "v_exp_f32_e32 v143, v143\n"
      "v_mfma_f32_16x16x32_bf16 v[132:135], v[116:119], a[132:135], v[132:135]\n"
      "ds_read_b128 a[188:191], v245 offset:7168\n"
      "v_cvt_pk_bf16_f32 v171, v142, v143\n"
      "v_mfma_f32_16x16x32_bf16 v[132:135], v[112:115], a[128:131], v[132:135]\n"
      "v_exp_f32_e32 v128, v128\n"
      "v_mfma_f32_16x16x32_bf16 v[176:179], a[96:99], a[160:163], v[176:179]\n"
      "ds_write_b128 v242, v[168:171] offset:13312\n"
      "v_mfma_f32_16x16x32_bf16 v[176:179], a[104:107], a[164:167], v[176:179]\n"
      "v_exp_f32_e32 v129, v129\n"
      "v_mfma_f32_16x16x32_bf16 v[176:179], a[112:115], a[168:171], v[176:179]\n"
      "v_mul_f32_e32 v200, v233, v200\n"
      "v_mul_f32_e32 v201, v233, v201\n"
      "v_mfma_f32_16x16x32_bf16 v[176:179], a[120:123], a[172:175], v[176:179]\n"
      "v_exp_f32_e32 v130, v130\n"
      "v_mfma_f32_16x16x32_bf16 v[180:183], a[124:127], a[172:175], v[180:183]\n"
      "v_mul_f32_e32 v202, v233, v202\n"
      "v_mul_f32_e32 v203, v233, v203\n"
      "v_mfma_f32_16x16x32_bf16 v[180:183], a[116:119], a[168:171], v[180:183]\n"
      ".Lst4_%=:\n"
      "s_cbranch_execz .Lstback_%=\n"
      "v_exp_f32_e32 v131, v131\n"
      "v_mfma_f32_16x16x32_bf16 v[180:183], a[108:111], a[164:167], v[180:183]\n"
      "v_cvt_pk_bf16_f32 v212, v200, v201\n"
      "v_cvt_pk_bf16_f32 v213, v202, v203\n"
      "v_mfma_f32_16x16x32_bf16 v[180:183], a[100:103], a[160:163], v[180:183]\n"
      "s_waitcnt lgkmcnt(1)\n"
      "v_exp_f32_e32 v132, v132\n"
      "v_mfma_f32_16x16x32_bf16 v[136:139], v[96:99], a[144:147], 0\n"
      "ds_read_b128 a[128:131], v240 offset:0\n"
      "v_cvt_pk_bf16_f32 v156, v128, v129\n"
      "v_mfma_f32_16x16x32_bf16 v[136:139], v[100:103], a[148:151], v[136:139]\n"
      "v_exp_f32_e32 v133, v133\n"
      "v_mfma_f32_16x16x32_bf16 v[136:139], v[104:107], a[152:155], v[136:139]\n"
      "ds_read_b128 a[132:135], v240 offset:256\n"
      "v_cvt_pk_bf16_f32 v157, v130, v131\n"
      "v_mfma_f32_16x16x32_bf16 v[136:139], v[108:111], a[156:159], v[136:139]\n"
      "v_exp_f32_e32 v134, v134\n"
      "v_mfma_f32_16x16x32_bf16 v[140:143], v[124:127], a[156:159], 0\n"
      "ds_read_b128 a[136:139], v240 offset:512\n"
      "v_cvt_pk_bf16_f32 v158, v132, v133\n"
      "v_mfma_f32_16x16x32_bf16 v[140:143], v[120:123], a[152:155], v[140:143]\n"
      "v_exp_f32_e32 v135, v135\n"
      "v_mfma_f32_16x16x32_bf16 v[140:143], v[116:119], a[148:151], v[140:143]\n"
      "ds_read_b128 a[140:143], v240 offset:768\n"
      "v_cvt_pk_bf16_f32 v159, v134, v135\n"
      "v_mfma_f32_16x16x32_bf16 v[140:143], v[112:115], a[144:147], v[140:143]\n"
      "v_exp_f32_e32 v136, v136\n"
      "v_smfmac_f32_16x16x64_bf16 v[224:227], v[216:219], v[152:159], v241\n"
      "ds_write_b128 v242, v[156:159] offset:14336\n"
      "v_mfma_f32_16x16x32_bf16 v[184:187], a[96:99], a[176:179], v[184:187]\n"
      "v_mul_f32_e32 v204, v233, v204\n"
      "v_mul_f32_e32 v205, v233, v205\n"
      "v_mfma_f32_16x16x32_bf16 v[184:187], a[104:107], a[180:183], v[184:187]\n"
      "v_exp_f32_e32 v137, v137\n"
      "v_mfma_f32_16x16x32_bf16 v[184:187], a[112:115], a[184:187], v[184:187]\n"
      "v_mul_f32_e32 v206, v233, v206\n"
      "v_mul_f32_e32 v207, v233, v207\n"
      "v_mfma_f32_16x16x32_bf16 v[184:187], a[120:123], a[188:191], v[184:187]\n"
      "v_exp_f32_e32 v138, v138\n"
      "v_mfma_f32_16x16x32_bf16 v[188:191], a[124:127], a[188:191], v[188:191]\n"
      "v_cvt_pk_bf16_f32 v214, v204, v205\n"
      "v_cvt_pk_bf16_f32 v215, v206, v207\n"
      "v_mfma_f32_16x16x32_bf16 v[188:191], a[116:119], a[184:187], v[188:191]\n"
      "v_exp_f32_e32 v139, v139\n"
      "v_mfma_f32_16x16x32_bf16 v[188:191], a[108:111], a[180:183], v[188:191]\n"
      "buffer_store_dwordx4 v[212:215], v250, s[52:55], s59 offen offset:0 sc0 sc1\n"
      "v_mfma_f32_16x16x32_bf16 v[188:191], a[100:103], a[176:179], v[188:191]\n"
      "s_waitcnt lgkmcnt(1)\n"
      "v_exp_f32_e32 v140, v140\n"
      ".LloopB1_%=:\n"
      "v_mfma_f32_16x16x32_bf16 v[128:131], v[0:3], a[128:131], 0\n"
      "ds_read_b128 a[144:147], v240 offset:4096\n"
      "v_cvt_pk_bf16_f32 v172, v136, v137\n"
      "v_mfma_f32_16x16x32_bf16 v[128:131], v[4:7], a[132:135], v[128:131]\n"
      "v_exp_f32_e32 v141, v141\n"
      "v_mfma_f32_16x16x32_bf16 v[128:131], v[8:11], a[136:139], v[128:131]\n"
      "ds_read_b128 a[148:151], v240 offset:4352\n"
      "v_cvt_pk_bf16_f32 v173, v138, v139\n"
      "v_mfma_f32_16x16x32_bf16 v[128:131], v[12:15], a[140:143], v[128:131]\n"
      "v_exp_f32_e32 v142, v142\n"
      "v_mfma_f32_16x16x32_bf16 v[132:135], v[28:31], a[140:143], 0\n"
      "ds_read_b128 a[152:155], v240 offset:4608\n"
      "v_cvt_pk_bf16_f32 v174, v140, v141\n"
      "v_mfma_f32_16x16x32_bf16 v[132:135], v[24:27], a[136:139], v[132:135]\n"
      "v_exp_f32_e32 v143, v143\n"
      "v_mfma_f32_16x16x32_bf16 v[132:135], v[20:23], a[132:135], v[132:135]\n"
      "ds_read_b128 a[156:159], v240 offset:4864\n"
      "v_cvt_pk_bf16_f32 v175, v142, v143\n"
      "v_mfma_f32_16x16x32_bf16 v[132:135], v[16:19], a[128:131], v[132:135]\n"
      "v_exp_f32_e32 v128, v128\n"
      "v_smfmac_f32_16x16x64_bf16 v[224:227], v[220:223], v[168:175], v241\n"
      "ds_write_b128 v242, v[172:175] offset:15360\n"
      "v_mfma_f32_16x16x32_bf16 v[192:195], a[0:3], v[144:147], 0\n"
      "v_mfma_f32_16x16x32_bf16 v[192:195], a[8:11], v[148:151], v[192:195]\n"
      "v_exp_f32_e32 v129, v129\n"
      "v_mfma_f32_16x16x32_bf16 v[192:195], a[16:19], v[152:155], v[192:195]\n"
      "v_mfma_f32_16x16x32_bf16 v[192:195], a[24:27], v[156:159], v[192:195]\n"
      "v_exp_f32_e32 v130, v130\n"
      "v_mfma_f32_16x16x32_bf16 v[196:199], a[28:31], v[156:159], 0\n"
      "v_add_f32_e32 v235, v224, v225\n"
      "v_mov_b32_e32 v224, s28\n"
      "v_mfma_f32_16x16x32_bf16 v[196:199], a[20:23], v[152:155], v[196:199]\n"
      "v_exp_f32_e32 v131, v131\n"
      "v_mfma_f32_16x16x32_bf16 v[196:199], a[12:15], v[148:151], v[196:199]\n"
      "s_waitcnt vmcnt(10) lgkmcnt(0)\n"
      "s_barrier\n"
      "v_mfma_f32_16x16x32_bf16 v[196:199], a[4:7], v[144:147], v[196:199]\n"
      "v_exp_f32_e32 v132, v132\n"
      "v_mfma_f32_16x16x32_bf16 v[136:139], v[0:3], a[144:147], 0\n"
      "ds_read_b128 a[160:163], v243 offset:8192\n"
      "v_cvt_pk_bf16_f32 v144, v128, v129\n"
      "v_mfma_f32_16x16x32_bf16 v[136:139], v[4:7], a[148:151], v[136:139]\n"
      "s_add_u32 s58, s58, 0x2000\n"
      "v_exp_f32_e32 v133, v133\n"
      "v_mfma_f32_16x16x32_bf16 v[136:139], v[8:11], a[152:155], v[136:139]\n"
      "ds_read_b128 a[164:167], v243 offset:10240\n"
      "v_cvt_pk_bf16_f32 v145, v130, v131\n"
      "v_mfma_f32_16x16x32_bf16 v[136:139], v[12:15], a[156:159], v[136:139]\n"
      "s_min_u32 s58, s58, s67\n"
      "v_exp_f32_e32 v134, v134\n"
      "v_mfma_f32_16x16x32_bf16 v[140:143], v[28:31], a[156:159], 0\n"
      "ds_read_b128 a[168:171], v243 offset:12288\n"
      "v_cvt_pk_bf16_f32 v146, v132, v133\n"
      "v_mfma_f32_16x16x32_bf16 v[140:143], v[24:27], a[152:155], v[140:143]\n"
      "s_xor_b32 m0, m0, 0x6000\n"
      "v_exp_f32_e32 v135, v135\n"
      "v_mfma_f32_16x16x32_bf16 v[140:143], v[20:23], a[148:151], v[140:143]\n"
      "ds_read_b128 a[172:175], v243 offset:14336\n"
      "v_cvt_pk_bf16_f32 v147, v134, v135\n"
      "v_mfma_f32_16x16x32_bf16 v[140:143], v[16:19], a[144:147], v[140:143]\n"
      "v_exp_f32_e32 v136, v136\n"
      "v_mfma_f32_16x16x32_bf16 v[200:203], a[0:3], v[160:163], 0\n"
      "ds_write_b128 v242, v[144:147] offset:0\n"
      "v_mfma_f32_16x16x32_bf16 v[200:203], a[8:11], v[164:167], v[200:203]\n"
      "v_exp_f32_e32 v137, v137\n"
      "v_mfma_f32_16x16x32_bf16 v[200:203], a[16:19], v[168:171], v[200:203]\n"
      "buffer_load_dwordx4 v248, s[40:43], s58 offen offset:0 lds\n"
      "v_mov_b32_e32 v225, 0\n"
      "v_mfma_f32_16x16x32_bf16 v[200:203], a[24:27], v[172:175], v[200:203]\n"
      "v_exp_f32_e32 v138, v138\n"
      "v_mfma_f32_16x16x32_bf16 v[204:207], a[28:31], v[172:175], 0\n"
      "ds_read_b128 v[228:231], v247 offset:0\n"
      "v_mfma_f32_16x16x32_bf16 v[204:207], a[20:23], v[168:171], v[204:207]\n"
      "v_exp_f32_e32 v139, v139\n"
      "v_mfma_f32_16x16x32_bf16 v[204:207], a[12:15], v[164:167], v[204:207]\n"
      "ds_write_b32 v246, v235 offset:1024\n"
      "v_mfma_f32_16x16x32_bf16 v[204:207], a[4:7], v[160:163], v[204:207]\n"
      "s_waitcnt lgkmcnt(3)\n"
      "v_exp_f32_e32 v140, v140\n"
      "v_mfma_f32_16x16x32_bf16 v[128:131], v[32:35], a[128:131], 0\n"
      "ds_read_b128 a[176:179], v243 offset:9216\n"
      "v_cvt_pk_bf16_f32 v160, v136, v137\n"
      "v_mfma_f32_16x16x32_bf16 v[128:131], v[36:39], a[132:135], v[128:131]\n"
      "v_exp_f32_e32 v141, v141\n"
      "v_mfma_f32_16x16x32_bf16 v[128:131], v[40:43], a[136:139], v[128:131]\n"
      "ds_read_b128 a[180:183], v243 offset:11264\n"
      "v_cvt_pk_bf16_f32 v161, v138, v139\n"
      "v_mfma_f32_16x16x32_bf16 v[128:131], v[44:47], a[140:143], v[128:131]\n"
      "v_exp_f32_e32 v142, v142\n"
      "v_mfma_f32_16x16x32_bf16 v[132:135], v[60:63], a[140:143], 0\n"
      "ds_read_b128 a[184:187], v243 offset:13312\n"
      "v_cvt_pk_bf16_f32 v162, v140, v141\n"
      "v_mfma_f32_16x16x32_bf16 v[132:135], v[56:59], a[136:139], v[132:135]\n"
      "v_exp_f32_e32 v143, v143\n"
      "v_mfma_f32_16x16x32_bf16 v[132:135], v[52:55], a[132:135], v[132:135]\n"
      "ds_read_b128 a[188:191], v243 offset:15360\n"
      "v_cvt_pk_bf16_f32 v163, v142, v143\n"
      "v_mfma_f32_16x16x32_bf16 v[132:135], v[48:51], a[128:131], v[132:135]\n"
      "s_waitcnt lgkmcnt(5)\n"
      "v_exp_f32_e32 v128, v128\n"
      "v_mfma_f32_16x16x32_bf16 v[192:195], a[32:35], a[160:163], v[192:195]\n"
      "ds_write_b128 v242, v[160:163] offset:1024\n"
      "v_mfma_f32_16x16x32_bf16 v[192:195], a[40:43], a[164:167], v[192:195]\n"
      "v_exp_f32_e32 v129, v129\n"
      "v_mfma_f32_16x16x32_bf16 v[192:195], a[48:51], a[168:171], v[192:195]\n"
      "buffer_load_dwordx4 v248, s[40:43], s58 offen offset:1024 lds\n"
      "v_add_f32_e32 v228, v228, v229\n"
      "v_mfma_f32_16x16x32_bf16 v[192:195], a[56:59], a[172:175], v[192:195]\n"
      "v_exp_f32_e32 v130, v130\n"
      "v_mfma_f32_16x16x32_bf16 v[196:199], a[60:63], a[172:175], v[196:199]\n"
      "v_add_f32_e32 v230, v230, v231\n"
      "v_add_f32_e32 v234, v228, v230\n"
      "v_mfma_f32_16x16x32_bf16 v[196:199], a[52:55], a[168:171], v[196:199]\n"
      "v_exp_f32_e32 v131, v131\n"
      "v_mfma_f32_16x16x32_bf16 v[196:199], a[44:47], a[164:167], v[196:199]\n"
      "v_rcp_f32_e32 v232, v234\n"
      "v_mfma_f32_16x16x32_bf16 v[196:199], a[36:39], a[160:163], v[196:199]\n"
      "s_waitcnt lgkmcnt(1)\n"
      "v_exp_f32_e32 v132, v132\n"
      "v_mfma_f32_16x16x32_bf16 v[136:139], v[32:35], a[144:147], 0\n"
      "ds_read_b128 a[160:163], v244 offset:8192\n"
      "v_cvt_pk_bf16_f32 v148, v128, v129\n"
      "v_mfma_f32_16x16x32_bf16 v[136:139], v[36:39], a[148:151], v[136:139]\n"
      "v_exp_f32_e32 v133, v133\n"
      "v_mfma_f32_16x16x32_bf16 v[136:139], v[40:43], a[152:155], v[136:139]\n"
      "ds_read_b128 a[164:167], v244 offset:10240\n"
      "v_cvt_pk_bf16_f32 v149, v130, v131\n"
      "v_mfma_f32_16x16x32_bf16 v[136:139], v[44:47], a[156:159], v[136:139]\n"
      "v_exp_f32_e32 v134, v134\n"
      "v_mfma_f32_16x16x32_bf16 v[140:143], v[60:63], a[156:159], 0\n"
      "ds_read_b128 a[168:171], v244 offset:12288\n"
      "v_cvt_pk_bf16_f32 v150, v132, v133\n"
      "v_mfma_f32_16x16x32_bf16 v[140:143], v[56:59], a[152:155], v[140:143]\n"
      "v_exp_f32_e32 v135, v135\n"
      "v_mfma_f32_16x16x32_bf16 v[140:143], v[52:55], a[148:151], v[140:143]\n"
      "ds_read_b128 a[172:175], v244 offset:14336\n"
      "v_cvt_pk_bf16_f32 v151, v134, v135\n"
      "v_mfma_f32_16x16x32_bf16 v[140:143], v[48:51], a[144:147], v[140:143]\n"
      "v_exp_f32_e32 v136, v136\n"
      "v_smfmac_f32_16x16x64_bf16 v[224:227], v[216:219], v[144:151], v241\n"
      "ds_write_b128 v242, v[148:151] offset:2048\n"
      "v_mfma_f32_16x16x32_bf16 v[200:203], a[32:35], a[176:179], v[200:203]\n"
      "s_add_u32 s59, s59, 0x2000\n"
      "v_mov_b32_e32 v233, v232\n"
      "v_med3_f32 v229, v234, 1.0, s63\n"
      "v_mfma_f32_16x16x32_bf16 v[200:203], a[40:43], a[180:183], v[200:203]\n"
      "v_exp_f32_e32 v137, v137\n"
      "v_mfma_f32_16x16x32_bf16 v[200:203], a[48:51], a[184:187], v[200:203]\n"
      "v_permlane32_swap_b32_e32 v232, v233\n"
      "v_mfma_f32_16x16x32_bf16 v[200:203], a[56:59], a[188:191], v[200:203]\n"
      "v_exp_f32_e32 v138, v138\n"
      "v_mfma_f32_16x16x32_bf16 v[204:207], a[60:63], a[188:191], v[204:207]\n"
      "v_cmp_neq_f32_e32 vcc, v229, v234\n"
      "v_mul_f32_e32 v176, v232, v176\n"
      "v_mfma_f32_16x16x32_bf16 v[204:207], a[52:55], a[184:187], v[204:207]\n"
      "v_exp_f32_e32 v139, v139\n"
      "v_mfma_f32_16x16x32_bf16 v[204:207], a[44:47], a[180:183], v[204:207]\n"
      "v_mul_f32_e32 v177, v232, v177\n"
      "v_mul_f32_e32 v178, v232, v178\n"
      "v_mfma_f32_16x16x32_bf16 v[204:207], a[36:39], a[176:179], v[204:207]\n"
      "s_waitcnt lgkmcnt(1)\n"
      "v_exp_f32_e32 v140, v140\n"
      "v_mfma_f32_16x16x32_bf16 v[128:131], v[64:67], a[128:131], 0\n"
      "ds_read_b128 a[176:179], v244 offset:9216\n"
      "v_cvt_pk_bf16_f32 v164, v136, v137\n"
      "v_mfma_f32_16x16x32_bf16 v[128:131], v[68:71], a[132:135], v[128:131]\n"
      "s_cmp_lg_u64 vcc, 0\n"
      "v_exp_f32_e32 v141, v141\n"
      "v_mfma_f32_16x16x32_bf16 v[128:131], v[72:75], a[136:139], v[128:131]\n"
      "ds_read_b128 a[180:183], v244 offset:11264\n"
      "v_cvt_pk_bf16_f32 v165, v138, v139\n"
      "v_mfma_f32_16x16x32_bf16 v[128:131], v[76:79], a[140:143], v[128:131]\n"
      "s_addc_u32 s66, s66, s66\n"
      "v_exp_f32_e32 v142, v142\n"
      "v_mfma_f32_16x16x32_bf16 v[132:135], v[92:95], a[140:143], 0\n"
      "ds_read_b128 a[184:187], v244 offset:13312\n"
      "v_cvt_pk_bf16_f32 v166, v140, v141\n"
      "v_mfma_f32_16x16x32_bf16 v[132:135], v[88:91], a[136:139], v[132:135]\n"
      "v_exp_f32_e32 v143, v143\n"
      "v_mfma_f32_16x16x32_bf16 v[132:135], v[84:87], a[132:135], v[132:135]\n"
      "ds_read_b128 a[188:191], v244 offset:15360\n"
      "v_cvt_pk_bf16_f32 v167, v142, v143\n"
      "v_mfma_f32_16x16x32_bf16 v[132:135], v[80:83], a[128:131], v[132:135]\n"
      "v_exp_f32_e32 v128, v128\n"
      "v_smfmac_f32_16x16x64_bf16 v[224:227], v[220:223], v[160:167], v241\n"
      "ds_write_b128 v242, v[164:167] offset:3072\n"
      "v_mfma_f32_16x16x32_bf16 v[192:195], a[64:67], a[160:163], v[192:195]\n"
      "v_mul_f32_e32 v179, v232, v179\n"
      "v_cvt_pk_bf16_f32 v208, v176, v177\n"
      "v_mfma_f32_16x16x32_bf16 v[192:195], a[72:75], a[164:167], v[192:195]\n"
      "v_exp_f32_e32 v129, v129\n"
      "v_mfma_f32_16x16x32_bf16 v[192:195], a[80:83], a[168:171], v[192:195]\n"
      "v_cvt_pk_bf16_f32 v209, v178, v179\n"
      "v_mfma_f32_16x16x32_bf16 v[192:195], a[88:91], a[172:175], v[192:195]\n"
      "v_exp_f32_e32 v130, v130\n"
      "v_mfma_f32_16x16x32_bf16 v[196:199], a[92:95], a[172:175], v[196:199]\n"
      "v_mul_f32_e32 v180, v232, v180\n"
      "v_mul_f32_e32 v181, v232, v181\n"
      "v_mfma_f32_16x16x32_bf16 v[196:199], a[84:87], a[168:171], v[196:199]\n"
      "v_exp_f32_e32 v131, v131\n"
      "v_mfma_f32_16x16x32_bf16 v[196:199], a[76:79], a[164:167], v[196:199]\n"
      "v_mul_f32_e32 v182, v232, v182\n"
      "v_mul_f32_e32 v183, v232, v183\n"
      "v_mfma_f32_16x16x32_bf16 v[196:199], a[68:71], a[160:163], v[196:199]\n"
      "s_waitcnt lgkmcnt(1)\n"
      "v_exp_f32_e32 v132, v132\n"
      "v_mfma_f32_16x16x32_bf16 v[136:139], v[64:67], a[144:147], 0\n"
      "ds_read_b128 a[160:163], v245 offset:8192\n"
      "v_cvt_pk_bf16_f32 v152, v128, v129\n"
      "v_mfma_f32_16x16x32_bf16 v[136:139], v[68:71], a[148:151], v[136:139]\n"
      "v_exp_f32_e32 v133, v133\n"
      "v_mfma_f32_16x16x32_bf16 v[136:139], v[72:75], a[152:155], v[136:139]\n"
      "ds_read_b128 a[164:167], v245 offset:10240\n"
      "v_cvt_pk_bf16_f32 v153, v130, v131\n"
      "v_mfma_f32_16x16x32_bf16 v[136:139], v[76:79], a[156:159], v[136:139]\n"
      "v_exp_f32_e32 v134, v134\n"
      "v_mfma_f32_16x16x32_bf16 v[140:143], v[92:95], a[156:159], 0\n"
      "ds_read_b128 a[168:171], v245 offset:12288\n"
      "v_cvt_pk_bf16_f32 v154, v132, v133\n"
      "v_mfma_f32_16x16x32_bf16 v[140:143], v[88:91], a[152:155], v[140:143]\n"
      "v_exp_f32_e32 v135, v135\n"
      "v_mfma_f32_16x16x32_bf16 v[140:143], v[84:87], a[148:151], v[140:143]\n"
      "ds_read_b128 a[172:175], v245 offset:14336\n"
      "v_cvt_pk_bf16_f32 v155, v134, v135\n"
      "v_mfma_f32_16x16x32_bf16 v[140:143], v[80:83], a[144:147], v[140:143]\n"
      "v_exp_f32_e32 v136, v136\n"
      "v_mfma_f32_16x16x32_bf16 v[200:203], a[64:67], a[176:179], v[200:203]\n"
      "ds_write_b128 v242, v[152:155] offset:4096\n"
      "v_mfma_f32_16x16x32_bf16 v[200:203], a[72:75], a[180:183], v[200:203]\n"
      "v_exp_f32_e32 v137, v137\n"
      "v_mfma_f32_16x16x32_bf16 v[200:203], a[80:83], a[184:187], v[200:203]\n"
      "v_cvt_pk_bf16_f32 v210, v180, v181\n"
      "v_mfma_f32_16x16x32_bf16 v[200:203], a[88:91], a[188:191], v[200:203]\n"
      "v_exp_f32_e32 v138, v138\n"
      "v_mfma_f32_16x16x32_bf16 v[204:207], a[92:95], a[188:191], v[204:207]\n"
      "v_cvt_pk_bf16_f32 v211, v182, v183\n"
      "v_mfma_f32_16x16x32_bf16 v[204:207], a[84:87], a[184:187], v[204:207]\n"
      "v_exp_f32_e32 v139, v139\n"
      "v_mfma_f32_16x16x32_bf16 v[204:207], a[76:79], a[180:183], v[204:207]\n"
      "buffer_store_dwordx4 v[208:211], v249, s[52:55], s59 offen offset:0 sc0 sc1\n"
      "v_mfma_f32_16x16x32_bf16 v[204:207], a[68:71], a[176:179], v[204:207]\n"
      "s_waitcnt lgkmcnt(1)\n"
      "v_exp_f32_e32 v140, v140\n"
      "v_mfma_f32_16x16x32_bf16 v[128:131], v[96:99], a[128:131], 0\n"
      "ds_read_b128 a[176:179], v245 offset:9216\n"
      "v_cvt_pk_bf16_f32 v168, v136, v137\n"
      "v_mfma_f32_16x16x32_bf16 v[128:131], v[100:103], a[132:135], v[128:131]\n"
      "v_exp_f32_e32 v141, v141\n"
      "v_mfma_f32_16x16x32_bf16 v[128:131], v[104:107], a[136:139], v[128:131]\n"
      "ds_read_b128 a[180:183], v245 offset:11264\n"
      "v_cvt_pk_bf16_f32 v169, v138, v139\n"
      "v_mfma_f32_16x16x32_bf16 v[128:131], v[108:111], a[140:143], v[128:131]\n"
      "v_exp_f32_e32 v142, v142\n"
      "v_mfma_f32_16x16x32_bf16 v[132:135], v[124:127], a[140:143], 0\n"
      "ds_read_b128 a[184:187], v245 offset:13312\n"
      "v_cvt_pk_bf16_f32 v170, v140, v141\n"
      "v_mfma_f32_16x16x32_bf16 v[132:135], v[120:123], a[136:139], v[132:135]\n"
      "v_exp_f32_e32 v143, v143\n"
      "v_mfma_f32_16x16x32_bf16 v[132:135], v[116:119], a[132:135], v[132:135]\n"
      "ds_read_b128 a[188:191], v245 offset:15360\n"
      "v_cvt_pk_bf16_f32 v171, v142, v143\n"
      "v_mfma_f32_16x16x32_bf16 v[132:135], v[112:115], a[128:131], v[132:135]\n"
      "v_exp_f32_e32 v128, v128\n"
      "v_mfma_f32_16x16x32_bf16 v[192:195], a[96:99], a[160:163], v[192:195]\n"
      "ds_write_b128 v242, v[168:171] offset:5120\n"
      "v_mfma_f32_16x16x32_bf16 v[192:195], a[104:107], a[164:167], v[192:195]\n"
      "v_exp_f32_e32 v129, v129\n"
      "v_mfma_f32_16x16x32_bf16 v[192:195], a[112:115], a[168:171], v[192:195]\n"
      "v_mul_f32_e32 v184, v233, v184\n"
      "v_mul_f32_e32 v185, v233, v185\n"
      "v_mfma_f32_16x16x32_bf16 v[192:195], a[120:123], a[172:175], v[192:195]\n"
      "v_exp_f32_e32 v130, v130\n"
      "v_mfma_f32_16x16x32_bf16 v[196:199], a[124:127], a[172:175], v[196:199]\n"
      "v_mul_f32_e32 v186, v233, v186\n"
      "v_mul_f32_e32 v187, v233, v187\n"
      "v_mfma_f32_16x16x32_bf16 v[196:199], a[116:119], a[168:171], v[196:199]\n"
      "v_exp_f32_e32 v131, v131\n"
      "v_mfma_f32_16x16x32_bf16 v[196:199], a[108:111], a[164:167], v[196:199]\n"
      "v_cvt_pk_bf16_f32 v212, v184, v185\n"
      "v_cvt_pk_bf16_f32 v213, v186, v187\n"
      "v_mfma_f32_16x16x32_bf16 v[196:199], a[100:103], a[160:163], v[196:199]\n"
      "s_waitcnt lgkmcnt(1)\n"
      "v_exp_f32_e32 v132, v132\n"
      "v_mfma_f32_16x16x32_bf16 v[136:139], v[96:99], a[144:147], 0\n"
      "ds_read_b128 a[128:131], v240 offset:8192\n"
      "v_cvt_pk_bf16_f32 v156, v128, v129\n"
      "v_mfma_f32_16x16x32_bf16 v[136:139], v[100:103], a[148:151], v[136:139]\n"
      "v_exp_f32_e32 v133, v133\n"
      "v_mfma_f32_16x16x32_bf16 v[136:139], v[104:107], a[152:155], v[136:139]\n"
      "ds_read_b128 a[132:135], v240 offset:8448\n"
      "v_cvt_pk_bf16_f32 v157, v130, v131\n"
      "v_mfma_f32_16x16x32_bf16 v[136:139], v[108:111], a[156:159], v[136:139]\n"
      "v_exp_f32_e32 v134, v134\n"
      "v_mfma_f32_16x16x32_bf16 v[140:143], v[124:127], a[156:159], 0\n"
      "ds_read_b128 a[136:139], v240 offset:8704\n"
      "v_cvt_pk_bf16_f32 v158, v132, v133\n"
      "v_mfma_f32_16x16x32_bf16 v[140:143], v[120:123], a[152:155], v[140:143]\n"
      "v_exp_f32_e32 v135, v135\n"
      "v_mfma_f32_16x16x32_bf16 v[140:143], v[116:119], a[148:151], v[140:143]\n"
      "ds_read_b128 a[140:143], v240 offset:8960\n"
      "v_cvt_pk_bf16_f32 v159, v134, v135\n"
      "v_mfma_f32_16x16x32_bf16 v[140:143], v[112:115], a[144:147], v[140:143]\n"
      "v_exp_f32_e32 v136, v136\n"
      "v_smfmac_f32_16x16x64_bf16 v[224:227], v[216:219], v[152:159], v241\n"
      "ds_write_b128 v242, v[156:159] offset:6144\n"
      "v_mfma_f32_16x16x32_bf16 v[200:203], a[96:99], a[176:179], v[200:203]\n"
      "v_mul_f32_e32 v188, v233, v188\n"
      "v_mul_f32_e32 v189, v233, v189\n"
      "v_mfma_f32_16x16x32_bf16 v[200:203], a[104:107], a[180:183], v[200:203]\n"
      "v_exp_f32_e32 v137, v137\n"
      "v_mfma_f32_16x16x32_bf16 v[200:203], a[112:115], a[184:187], v[200:203]\n"
      "v_mul_f32_e32 v190, v233, v190\n"
      "v_mul_f32_e32 v191, v233, v191\n"
      "v_mfma_f32_16x16x32_bf16 v[200:203], a[120:123], a[188:191], v[200:203]\n"
      "s_sub_u32 s61, s61, 1\n"
      "v_exp_f32_e32 v138, v138\n"
      "v_mfma_f32_16x16x32_bf16 v[204:207], a[124:127], a[188:191], v[204:207]\n"
      "v_cvt_pk_bf16_f32 v214, v188, v189\n"
      "v_cvt_pk_bf16_f32 v215, v190, v191\n"
      "v_mfma_f32_16x16x32_bf16 v[204:207], a[116:119], a[184:187], v[204:207]\n"
      "s_cmp_lg_u32 s61, 0\n"
      "v_exp_f32_e32 v139, v139\n"
      "v_mfma_f32_16x16x32_bf16 v[204:207], a[108:111], a[180:183], v[204:207]\n"
      "buffer_store_dwordx4 v[212:215], v250, s[52:55], s59 offen offset:0 sc0 sc1\n"
      "v_mfma_f32_16x16x32_bf16 v[204:207], a[100:103], a[176:179], v[204:207]\n"
      "s_waitcnt lgkmcnt(1)\n"
      "v_exp_f32_e32 v140, v140\n"
      "s_cbranch_scc1 .Lloop1_%=\n"
      ".Ltail_%=:\n"
      "v_mfma_f32_16x16x32_bf16 v[176:179], a[0:3], v[144:147], 0\n"
      "v_exp_f32_e32 v141, v141\n"
      "v_mfma_f32_16x16x32_bf16 v[176:179], a[8:11], v[148:151], v[176:179]\n"
      "v_exp_f32_e32 v142, v142\n"
      "v_mfma_f32_16x16x32_bf16 v[176:179], a[16:19], v[152:155], v[176:179]\n"
      "v_exp_f32_e32 v143, v143\n"
      "v_mfma_f32_16x16x32_bf16 v[176:179], a[24:27], v[156:159], v[176:179]\n"
      "v_cvt_pk_bf16_f32 v172, v136, v137\n"
      "v_cvt_pk_bf16_f32 v173, v138, v139\n"
      "v_mfma_f32_16x16x32_bf16 v[180:183], a[4:7], v[144:147], 0\n"
      "v_cvt_pk_bf16_f32 v174, v140, v141\n"
      "v_cvt_pk_bf16_f32 v175, v142, v143\n"
      "v_mfma_f32_16x16x32_bf16 v[180:183], a[12:15], v[148:151], v[180:183]\n"
      "ds_write_b128 v242, v[172:175] offset:7168\n"
      "v_mfma_f32_16x16x32_bf16 v[180:183], a[20:23], v[152:155], v[180:183]\n"
      "v_mfma_f32_16x16x32_bf16 v[180:183], a[28:31], v[156:159], v[180:183]\n"
      "v_smfmac_f32_16x16x64_bf16 v[224:227], v[220:223], v[168:175], v241\n"
      "v_mfma_f32_16x16x32_bf16 v[184:187], a[0:3], v[160:163], 0\n"
      "s_waitcnt lgkmcnt(0)\n"
      "s_barrier\n"
      "v_mfma_f32_16x16x32_bf16 v[184:187], a[8:11], v[164:167], v[184:187]\n"
      "ds_read_b128 a[160:163], v243 offset:0\n"
      "v_mfma_f32_16x16x32_bf16 v[184:187], a[16:19], v[168:171], v[184:187]\n"
      "ds_read_b128 a[164:167], v243 offset:2048\n"
      "v_mfma_f32_16x16x32_bf16 v[184:187], a[24:27], v[172:175], v[184:187]\n"
      "ds_read_b128 a[168:171], v243 offset:4096\n"
      "v_mfma_f32_16x16x32_bf16 v[188:191], a[4:7], v[160:163], 0\n"
      "ds_read_b128 a[172:175], v243 offset:6144\n"
      "v_mfma_f32_16x16x32_bf16 v[188:191], a[12:15], v[164:167], v[188:191]\n"
      "ds_read_b128 a[176:179], v244 offset:0\n"
      "v_add_f32_e32 v235, v224, v225\n"
      "v_mfma_f32_16x16x32_bf16 v[188:191], a[20:23], v[168:171], v[188:191]\n"
      "ds_read_b128 v[228:231], v247 offset:1024\n"
      "v_mfma_f32_16x16x32_bf16 v[188:191], a[28:31], v[172:175], v[188:191]\n"
      "ds_write_b32 v246, v235 offset:0\n"
      "s_waitcnt lgkmcnt(3)\n"
      "v_mfma_f32_16x16x32_bf16 v[176:179], a[32:35], a[160:163], v[176:179]\n"
      "ds_read_b128 a[180:183], v244 offset:2048\n"
      "v_mfma_f32_16x16x32_bf16 v[176:179], a[40:43], a[164:167], v[176:179]\n"
      "ds_read_b128 a[184:187], v244 offset:4096\n"
      "v_mfma_f32_16x16x32_bf16 v[176:179], a[48:51], a[168:171], v[176:179]\n"
      "ds_read_b128 a[188:191], v244 offset:6144\n"
      "v_mfma_f32_16x16x32_bf16 v[176:179], a[56:59], a[172:175], v[176:179]\n"
      "ds_read_b128 a[192:195], v245 offset:0\n"
      "v_mfma_f32_16x16x32_bf16 v[180:183], a[36:39], a[160:163], v[180:183]\n"
      "ds_read_b128 a[196:199], v245 offset:2048\n"
      "v_mfma_f32_16x16x32_bf16 v[180:183], a[44:47], a[164:167], v[180:183]\n"
      "ds_read_b128 a[200:203], v245 offset:4096\n"
      "v_mfma_f32_16x16x32_bf16 v[180:183], a[52:55], a[168:171], v[180:183]\n"
      "ds_read_b128 a[204:207], v245 offset:6144\n"
      "v_mfma_f32_16x16x32_bf16 v[180:183], a[60:63], a[172:175], v[180:183]\n"
      "ds_read_b128 a[208:211], v243 offset:1024\n"
      "s_waitcnt lgkmcnt(5)\n"
      "v_mfma_f32_16x16x32_bf16 v[176:179], a[64:67], a[176:179], v[176:179]\n"
      "ds_read_b128 a[212:215], v243 offset:3072\n"
      "v_mfma_f32_16x16x32_bf16 v[176:179], a[72:75], a[180:183], v[176:179]\n"
      "ds_read_b128 a[216:219], v243 offset:5120\n"
      "v_mfma_f32_16x16x32_bf16 v[176:179], a[80:83], a[184:187], v[176:179]\n"
      "ds_read_b128 a[220:223], v243 offset:7168\n"
      "s_waitcnt lgkmcnt(11)\n"
      "s_barrier\n"
      "v_mfma_f32_16x16x32_bf16 v[176:179], a[88:91], a[188:191], v[176:179]\n"
      "ds_read_b128 a[224:227], v244 offset:1024\n"
      "v_add_f32_e32 v228, v228, v229\n"
      "v_mfma_f32_16x16x32_bf16 v[180:183], a[68:71], a[176:179], v[180:183]\n"
      "ds_read_b128 a[228:231], v244 offset:3072\n"
      "v_add_f32_e32 v230, v230, v231\n"
      "v_mfma_f32_16x16x32_bf16 v[180:183], a[76:79], a[180:183], v[180:183]\n"
      "ds_read_b128 a[232:235], v244 offset:5120\n"
      "v_add_f32_e32 v234, v228, v230\n"
      "v_mfma_f32_16x16x32_bf16 v[180:183], a[84:87], a[184:187], v[180:183]\n"
      "s_add_u32 s59, s59, 0x2000\n"
      "ds_read_b128 a[236:239], v244 offset:7168\n"
      "v_rcp_f32_e32 v232, v234\n"
      "v_mfma_f32_16x16x32_bf16 v[180:183], a[92:95], a[188:191], v[180:183]\n"
      "ds_read_b128 a[240:243], v245 offset:1024\n"
      "s_waitcnt lgkmcnt(9)\n"
      "v_mfma_f32_16x16x32_bf16 v[176:179], a[96:99], a[192:195], v[176:179]\n"
      "ds_read_b128 a[244:247], v245 offset:3072\n"
      "v_mov_b32_e32 v233, v232\n"
      "v_mfma_f32_16x16x32_bf16 v[176:179], a[104:107], a[196:199], v[176:179]\n"
      "ds_read_b128 a[248:251], v245 offset:5120\n"
      "v_med3_f32 v229, v234, 1.0, s63\n"
      "v_mfma_f32_16x16x32_bf16 v[176:179], a[112:115], a[200:203], v[176:179]\n"
      "ds_read_b128 a[252:255], v245 offset:7168\n"
      "v_cmp_neq_f32_e32 vcc, v229, v234\n"
      "v_mfma_f32_16x16x32_bf16 v[176:179], a[120:123], a[204:207], v[176:179]\n"
      "ds_read_b128 v[228:231], v247 offset:0\n"
      "v_permlane32_swap_b32_e32 v232, v233\n"
      "v_mfma_f32_16x16x32_bf16 v[180:183], a[100:103], a[192:195], v[180:183]\n"
      "s_cmp_lg_u64 vcc, 0\n"
      "s_addc_u32 s66, s66, s66\n"
      "v_mfma_f32_16x16x32_bf16 v[180:183], a[108:111], a[196:199], v[180:183]\n"
      "v_mul_f32_e32 v192, v232, v192\n"
      "v_mul_f32_e32 v193, v232, v193\n"
      "v_mul_f32_e32 v194, v232, v194\n"
      "v_mul_f32_e32 v195, v232, v195\n"
      "v_mfma_f32_16x16x32_bf16 v[180:183], a[116:119], a[200:203], v[180:183]\n"
      "v_cvt_pk_bf16_f32 v208, v192, v193\n"
      "v_cvt_pk_bf16_f32 v209, v194, v195\n"
      "v_mul_f32_e32 v196, v232, v196\n"
      "v_mul_f32_e32 v197, v232, v197\n"
      "v_mfma_f32_16x16x32_bf16 v[180:183], a[124:127], a[204:207], v[180:183]\n"
      "s_waitcnt lgkmcnt(9)\n"
      "v_mul_f32_e32 v198, v232, v198\n"
      "v_mul_f32_e32 v199, v232, v199\n"
      "v_cvt_pk_bf16_f32 v210, v196, v197\n"
      "v_cvt_pk_bf16_f32 v211, v198, v199\n"
      "v_mfma_f32_16x16x32_bf16 v[184:187], a[32:35], a[208:211], v[184:187]\n"
      "buffer_store_dwordx4 v[208:211], v249, s[52:55], s59 offen offset:0 sc0 sc1\n"
      "v_mfma_f32_16x16x32_bf16 v[184:187], a[40:43], a[212:215], v[184:187]\n"
      "v_mul_f32_e32 v200, v233, v200\n"
      "v_mul_f32_e32 v201, v233, v201\n"
      "v_mul_f32_e32 v202, v233, v202\n"
      "v_mul_f32_e32 v203, v233, v203\n"
      "v_mfma_f32_16x16x32_bf16 v[184:187], a[48:51], a[216:219], v[184:187]\n"
      "v_cvt_pk_bf16_f32 v212, v200, v201\n"
      "v_cvt_pk_bf16_f32 v213, v202, v203\n"
      "v_mul_f32_e32 v204, v233, v204\n"
      "v_mul_f32_e32 v205, v233, v205\n"
      "v_mfma_f32_16x16x32_bf16 v[184:187], a[56:59], a[220:223], v[184:187]\n"
      "v_mul_f32_e32 v206, v233, v206\n"
      "v_mul_f32_e32 v207, v233, v207\n"
      "v_cvt_pk_bf16_f32 v214, v204, v205\n"
      "v_cvt_pk_bf16_f32 v215, v206, v207\n"
      "v_mfma_f32_16x16x32_bf16 v[188:191], a[36:39], a[208:211], v[188:191]\n"
      "buffer_store_dwordx4 v[212:215], v250, s[52:55], s59 offen offset:0 sc0 sc1\n"
      "v_mfma_f32_16x16x32_bf16 v[188:191], a[44:47], a[212:215], v[188:191]\n"
      "s_waitcnt lgkmcnt(0)\n"
      "v_mfma_f32_16x16x32_bf16 v[188:191], a[52:55], a[216:219], v[188:191]\n"
      "v_add_f32_e32 v228, v228, v229\n"
      "v_add_f32_e32 v230, v230, v231\n"
      "v_mfma_f32_16x16x32_bf16 v[188:191], a[60:63], a[220:223], v[188:191]\n"
      "s_waitcnt lgkmcnt(5)\n"
      "v_add_f32_e32 v234, v228, v230\n"
      "v_mfma_f32_16x16x32_bf16 v[184:187], a[64:67], a[224:227], v[184:187]\n"
      "s_add_u32 s59, s59, 0x2000\n"
      "v_rcp_f32_e32 v232, v234\n"
      "v_mfma_f32_16x16x32_bf16 v[184:187], a[72:75], a[228:231], v[184:187]\n"
      "v_mfma_f32_16x16x32_bf16 v[184:187], a[80:83], a[232:235], v[184:187]\n"
      "v_mov_b32_e32 v233, v232\n"
      "v_mfma_f32_16x16x32_bf16 v[184:187], a[88:91], a[236:239], v[184:187]\n"
      "v_med3_f32 v229, v234, 1.0, s63\n"
      "v_mfma_f32_16x16x32_bf16 v[188:191], a[68:71], a[224:227], v[188:191]\n"
      "v_cmp_neq_f32_e32 vcc, v229, v234\n"
      "v_mfma_f32_16x16x32_bf16 v[188:191], a[76:79], a[228:231], v[188:191]\n"
      "v_permlane32_swap_b32_e32 v232, v233\n"
      "v_mfma_f32_16x16x32_bf16 v[188:191], a[84:87], a[232:235], v[188:191]\n"
      "s_nop 0\n"
      "s_cmp_lg_u64 vcc, 0\n"
      "s_addc_u32 s66, s66, s66\n"
      "v_mfma_f32_16x16x32_bf16 v[188:191], a[92:95], a[236:239], v[188:191]\n"
      "s_waitcnt lgkmcnt(1)\n"
      "v_mul_f32_e32 v176, v232, v176\n"
      "v_mul_f32_e32 v177, v232, v177\n"
      "v_mfma_f32_16x16x32_bf16 v[184:187], a[96:99], a[240:243], v[184:187]\n"
      "v_mul_f32_e32 v178, v232, v178\n"
      "v_mfma_f32_16x16x32_bf16 v[184:187], a[104:107], a[244:247], v[184:187]\n"
      "v_mul_f32_e32 v179, v232, v179\n"
      "v_cvt_pk_bf16_f32 v208, v176, v177\n"
      "v_mfma_f32_16x16x32_bf16 v[184:187], a[112:115], a[248:251], v[184:187]\n"
      "v_cvt_pk_bf16_f32 v209, v178, v179\n"
      "v_mfma_f32_16x16x32_bf16 v[184:187], a[120:123], a[252:255], v[184:187]\n"
      "v_mul_f32_e32 v180, v232, v180\n"
      "v_mul_f32_e32 v181, v232, v181\n"
      "v_mfma_f32_16x16x32_bf16 v[188:191], a[100:103], a[240:243], v[188:191]\n"
      "v_mul_f32_e32 v182, v232, v182\n"
      "v_mfma_f32_16x16x32_bf16 v[188:191], a[108:111], a[244:247], v[188:191]\n"
      "v_mul_f32_e32 v183, v232, v183\n"
      "v_cvt_pk_bf16_f32 v210, v180, v181\n"
      "v_mfma_f32_16x16x32_bf16 v[188:191], a[116:119], a[248:251], v[188:191]\n"
      "v_cvt_pk_bf16_f32 v211, v182, v183\n"
      "v_mfma_f32_16x16x32_bf16 v[188:191], a[124:127], a[252:255], v[188:191]\n"
      "buffer_store_dwordx4 v[208:211], v249, s[52:55], s59 offen offset:0 sc0 sc1\n"
      "v_mul_f32_e32 v184, v233, v184\n"
      "v_mul_f32_e32 v185, v233, v185\n"
      "v_mul_f32_e32 v186, v233, v186\n"
      "v_mul_f32_e32 v187, v233, v187\n"
      "v_cvt_pk_bf16_f32 v212, v184, v185\n"
      "v_cvt_pk_bf16_f32 v213, v186, v187\n"
      "s_nop 0\n"
      "v_mul_f32_e32 v188, v233, v188\n"
      "v_mul_f32_e32 v189, v233, v189\n"
      "v_mul_f32_e32 v190, v233, v190\n"
      "v_mul_f32_e32 v191, v233, v191\n"
      "v_cvt_pk_bf16_f32 v214, v188, v189\n"
      "v_cvt_pk_bf16_f32 v215, v190, v191\n"
      "buffer_store_dwordx4 v[212:215], v250, s[52:55], s59 offen offset:0 sc0 sc1\n"
      "s_waitcnt lgkmcnt(0)\n"
      "s_cmp_lg_u32 s66, 0\n"
      "s_cbranch_scc1 .Lslow_%=\n"
      "s_endpgm\n"
      ".Lslow_%=:\n"
      "s_mov_b32 %0, s66\n"
      ".Lend_%=:\n"
      : "=s"(mask)
      : "s"(__builtin_amdgcn_kernarg_segment_ptr()), "s"(blockIdx.x), "s"(w)
      : "v0", "v1", "v2", "v3", "v4", "v5", "v6", "v7", "v8", "v9", "v10", "v11", "v12", "v13", "v14", "v15", "v16", "v17", "v18", "v19", "v20", "v21", "v22", "v23", "v24", "v25", "v26", "v27", "v28", "v29", "v30", "v31", "v32", "v33", "v34", "v35", "v36", "v37", "v38", "v39", "v40", "v41", "v42", "v43", "v44", "v45", "v46", "v47", "v48", "v49", "v50", "v51", "v52", "v53", "v54", "v55", "v56", "v57", "v58", "v59", "v60", "v61", "v62", "v63", "v64", "v65", "v66", "v67", "v68", "v69", "v70", "v71", "v72", "v73", "v74", "v75", "v76", "v77", "v78", "v79", "v80", "v81", "v82", "v83", "v84", "v85", "v86", "v87", "v88", "v89", "v90", "v91", "v92", "v93", "v94", "v95", "v96", "v97", "v98", "v99", "v100", "v101", "v102", "v103", "v104", "v105", "v106", "v107", "v108", "v109", "v110", "v111", "v112", "v113", "v114", "v115", "v116", "v117", "v118", "v119", "v120", "v121", "v122", "v123", "v124", "v125", "v126", "v127", "v128", "v129", "v130", "v131", "v132", "v133", "v134", "v135", "v136", "v137", "v138", "v139", "v140", "v141", "v142", "v143", "v144", "v145", "v146", "v147", "v148", "v149", "v150", "v151", "v152", "v153", "v154", "v155", "v156", "v157", "v158", "v159", "v160", "v161", "v162", "v163", "v164", "v165", "v166", "v167", "v168", "v169", "v170", "v171", "v172", "v173", "v174", "v175", "v176", "v177", "v178", "v179", "v180", "v181", "v182", "v183", "v184", "v185", "v186", "v187", "v188", "v189", "v190", "v191", "v192", "v193", "v194", "v195", "v196", "v197", "v198", "v199", "v200", "v201", "v202", "v203", "v204", "v205", "v206", "v207", "v208", "v209", "v210", "v211", "v212", "v213", "v214", "v215", "v216", "v217", "v218", "v219", "v220", "v221", "v222", "v223", "v224", "v225", "v226", "v227", "v228", "v229", "v230", "v231", "v232", "v233", "v234", "v235", "v236", "v237", "v238", "v239", "v240", "v241", "v242", "v243", "v244", "v245", "v246", "v247", "v248", "v249", "v250", "v251", "v252", "v253", "v254", "v255", "a0", "a1", "a2", "a3", "a4", "a5", "a6", "a7", "a8", "a9", "a10", "a11", "a12", "a13", "a14", "a15", "a16", "a17", "a18", "a19", "a20", "a21", "a22", "a23", "a24", "a25", "a26", "a27", "a28", "a29", "a30", "a31", "a32", "a33", "a34", "a35", "a36", "a37", "a38", "a39", "a40", "a41", "a42", "a43", "a44", "a45", "a46", "a47", "a48", "a49", "a50", "a51", "a52", "a53", "a54", "a55", "a56", "a57", "a58", "a59", "a60", "a61", "a62", "a63", "a64", "a65", "a66", "a67", "a68", "a69", "a70", "a71", "a72", "a73", "a74", "a75", "a76", "a77", "a78", "a79", "a80", "a81", "a82", "a83", "a84", "a85", "a86", "a87", "a88", "a89", "a90", "a91", "a92", "a93", "a94", "a95", "a96", "a97", "a98", "a99", "a100", "a101", "a102", "a103", "a104", "a105", "a106", "a107", "a108", "a109", "a110", "a111", "a112", "a113", "a114", "a115", "a116", "a117", "a118", "a119", "a120", "a121", "a122", "a123", "a124", "a125", "a126", "a127", "a128", "a129", "a130", "a131", "a132", "a133", "a134", "a135", "a136", "a137", "a138", "a139", "a140", "a141", "a142", "a143", "a144", "a145", "a146", "a147", "a148", "a149", "a150", "a151", "a152", "a153", "a154", "a155", "a156", "a157", "a158", "a159", "a160", "a161", "a162", "a163", "a164", "a165", "a166", "a167", "a168", "a169", "a170", "a171", "a172", "a173", "a174", "a175", "a176", "a177", "a178", "a179", "a180", "a181", "a182", "a183", "a184", "a185", "a186", "a187", "a188", "a189", "a190", "a191", "a192", "a193", "a194", "a195", "a196", "a197", "a198", "a199", "a200", "a201", "a202", "a203", "a204", "a205", "a206", "a207", "a208", "a209", "a210", "a211", "a212", "a213", "a214", "a215", "a216", "a217", "a218", "a219", "a220", "a221", "a222", "a223", "a224", "a225", "a226", "a227", "a228", "a229", "a230", "a231", "a232", "a233", "a234", "a235", "a236", "a237", "a238", "a239", "a240", "a241", "a242", "a243", "a244", "a245", "a246", "a247", "a248", "a249", "a250", "a251", "a252", "a253", "a254", "a255", "s12", "s13", "s14", "s15", "s16", "s17", "s18", "s19", "s20", "s21", "s22", "s23", "s24", "s25", "s26", "s27", "s28", "s29", "s35", "s36", "s37", "s38", "s39", "s40", "s41", "s42", "s43", "s44", "s45", "s46", "s47", "s48", "s49", "s50", "s51", "s52", "s53", "s54", "s55", "s56", "s57", "s58", "s59", "s60", "s61", "s62", "s63", "s64", "s65", "s66", "s67", "s68", "s69", "s70", "s71", "vcc", "scc", "memory");
  if (mask != 0u) {
    if ((mask & 0x07ffffffu) == 0x07ffffffu) mask = 0x7fffffffu;
    const unsigned b = blockIdx.x;
    const unsigned group = (b & 7u) * 16u + (b >> 4), half = (b >> 3) & 1u;
    const size_t row0 = (size_t)group * 1984u + half * 992u;
    const int lane = __builtin_amdgcn_mbcnt_hi(~0u, __builtin_amdgcn_mbcnt_lo(~0u, 0u));
    asm volatile("s_waitcnt vmcnt(0)" ::: "memory");
    __syncthreads();
    for (int blk = 0; blk < 31; ++blk)
      if ((mask >> (30 - blk)) & 1u)
        exact_block(Q + (row0 + blk * 32) * 128, K + (size_t)group * (512 * 128), V + (size_t)group * (512 * 128), O + (row0 + blk * 32) * 128, lds, w, lane);
  }
}

extern "C" void kernel_launch(void* const* d_in, const int* in_sizes, int n_in, void* d_out, int out_size, void* d_ws, size_t ws_size,
                              hipStream_t stream) {
  gqa_attn_kernel<<<256, 256, 0, stream>>>((const unsigned short*)d_in[0], (const unsigned short*)d_in[1], (const unsigned short*)d_in[2],
                                           (unsigned short*)d_out
  );
}
```
